```python
import torch

def _softmax(x, axis=-1):
    return torch.softmax(x, dim=axis)
def _gelu(x):
    return torch.nn.functional.gelu(x, approximate="tanh")
def _silu(x):
    return torch.nn.functional.silu(x)
def _sigmoid(x):
    return torch.sigmoid(x)
def _layernorm(x, axis=-1, eps=1e-5):
    m = x.mean(dim=axis, keepdim=True)
    v = x.var(dim=axis, keepdim=True, unbiased=False)
    return (x - m) / torch.sqrt(v + eps)
def _scatter_add(tgt, idx, src, axis=0):
    return tgt.clone().index_add_(axis, idx, src)
def _segment_sum(src, seg, n):
    out = torch.zeros((n,) + src.shape[1:], dtype=src.dtype)
    return out.index_add_(0, seg, src)
def _pad(x, pw):
    flat = tuple(v for pair in reversed(pw) for v in pair)
    return torch.nn.functional.pad(x, flat)

kernel_name = 'interleave_layers__linear_kernel_hedgehog__sliding_local_dilated__prompt'

def setup_inputs():

    _w = (lambda a: (lambda t: t.to(torch.bfloat16) if t.is_floating_point() else t)(torch.as_tensor(a)))
    def _A_setup():
        B, S, D, H = 1, 2048, 512, 32
        Dh = D // H
        return {
            "x": _w((lambda *s: torch.randn(*s, dtype=torch.bfloat16) * 0.05)(B, S, D)),
            "wqkv": _w((lambda *s: torch.randn(*s, dtype=torch.bfloat16) * 0.4)(D, 3 * D)),
            "w_phi": _w((lambda *s: torch.randn(*s, dtype=torch.bfloat16) * 0.4)(H, Dh, Dh) * (Dh ** -0.5)),
            "wo": _w((lambda *s: torch.randn(*s, dtype=torch.bfloat16) * 0.05)(D, D)),
        }
    _A_in = _A_setup()

    def _B_setup():
        B, S, D = 1, 2048, 512
        return {
            "x": _w((lambda *s: torch.randn(*s, dtype=torch.bfloat16) * 0.05)(B, S, D)),
            "wqkv": _w((lambda *s: torch.randn(*s, dtype=torch.bfloat16) * 0.1)(D, 3 * D)),
            "wo": _w((lambda *s: torch.randn(*s, dtype=torch.bfloat16) * 0.05)(D, D)),
        }
    _B_in = _B_setup()

    return {
        "A_x": _A_in["x"],
        "A_wqkv": _A_in["wqkv"],
        "A_w_phi": _A_in["w_phi"],
        "A_wo": _A_in["wo"],
        "B_x": _B_in["x"],
        "B_wqkv": _B_in["wqkv"],
        "B_wo": _B_in["wo"],
    }

def reference(A_x, A_wqkv, A_w_phi, A_wo, B_x, B_wqkv, B_wo):
    def _A_ref(x, wqkv, w_phi, wo):
        B, S, D = x.shape
        H = 32; Dh = D // H
        qkv = (lambda x, *d: x.permute(*d))((x @ wqkv).reshape(B, S, 3, H, Dh), 2, 0, 3, 1, 4)
        q, k, v = qkv[0], qkv[1], qkv[2]
        pq = torch.einsum("bhsd,hdf->bhsf", q, w_phi)
        pk = torch.einsum("bhsd,hdf->bhsf", k, w_phi)
        phi_q = torch.concatenate([_softmax(pq, axis=-1), _softmax(-pq, axis=-1)], axis=-1)
        phi_k = torch.concatenate([_softmax(pk, axis=-1), _softmax(-pk, axis=-1)], axis=-1)
        kv = torch.einsum("bhsf,bhsd->bhfd", phi_k, v)
        z  = torch.einsum("bhsf->bhf", phi_k)
        num = torch.einsum("bhqf,bhfd->bhqd", phi_q, kv)
        den = torch.einsum("bhqf,bhf->bhq", phi_q, z)[..., None] + 1e-6
        o = num / den
        return (lambda x, *d: x.permute(*d))(o, 0, 2, 1, 3).reshape(B, S, D) @ wo

    def _B_ref(x, wqkv, wo):
        B, S, D = x.shape
        H = 32; Dh = D // H
        W, dil = 8, 2
        qkv = (lambda x, *d: x.permute(*d))((x @ wqkv).reshape(B, S, 3, H, Dh), 2, 0, 3, 1, 4)
        q, k, v = qkv[0], qkv[1], qkv[2]
        scale = Dh ** -0.5
        pos = torch.arange(S)
        off = torch.arange(W) * dil
        idx = pos[:, None] - off[None, :]
        mask = idx >= 0
        idx_c = torch.where(mask, idx, 0).reshape(-1)
        kg = k[:, :, idx_c, :].reshape(B, H, S, W, Dh)
        vg = v[:, :, idx_c, :].reshape(B, H, S, W, Dh)
        logits = torch.einsum("bhsd,bhswd->bhsw", q, kg) * scale
        logits = torch.where(mask[None, None, :, :], logits, -1e9)
        attn = _softmax(logits, axis=-1)
        o = torch.einsum("bhsw,bhswd->bhsd", attn, vg)
        return (lambda x, *d: x.permute(*d))(o, 0, 2, 1, 3).reshape(B, S, D) @ wo

    x = A_x
    x = x + _A_ref(_layernorm(x, axis=-1), A_wqkv, A_w_phi, A_wo)
    x = x + _B_ref(_layernorm(x, axis=-1), B_wqkv, B_wo)
    x = x + _A_ref(_layernorm(x, axis=-1), A_wqkv, A_w_phi, A_wo)
    x = x + _B_ref(_layernorm(x, axis=-1), B_wqkv, B_wo)
    return x
```

```cpp
#include <hip/hip_runtime.h>
#include <hip/hip_bf16.h>
#include <stdint.h>

typedef unsigned short u16;
__device__ __forceinline__ float bf2f(u16 b) { return __uint_as_float(((unsigned)b) << 16); }
__device__ __forceinline__ u16 f2bf(float f) { __hip_bfloat16 h = __float2bfloat16(f); return *(u16*)&h; }

constexpr int S = 2048, D = 512, H = 32, DH = 16;

__global__ void k_ln(const u16* x, u16* xn) {
  int row = blockIdx.x; int t = threadIdx.x;
  float v[8]; float s = 0;
  for (int i = 0; i < 8; ++i) { v[i] = bf2f(x[row * D + t * 8 + i]); s += v[i]; }
  for (int o = 32; o; o >>= 1) s += __shfl_xor(s, o);
  float m = s / D; float q = 0;
  for (int i = 0; i < 8; ++i) { float d = v[i] - m; q += d * d; }
  for (int o = 32; o; o >>= 1) q += __shfl_xor(q, o);
  float r = rsqrtf(q / D + 1e-5f);
  for (int i = 0; i < 8; ++i) xn[row * D + t * 8 + i] = f2bf((v[i] - m) * r);
}
__global__ void k_gemm(const u16* A, const u16* B, const u16* res, u16* C, int M, int N, int K) {
  __shared__ float As[16][17], Bs[16][17];
  int tx = threadIdx.x & 15, ty = threadIdx.x >> 4;
  int row = blockIdx.y * 16 + ty, col = blockIdx.x * 16 + tx;
  float acc = 0;
  for (int k0 = 0; k0 < K; k0 += 16) {
    As[ty][tx] = bf2f(A[row * K + k0 + tx]);
    Bs[ty][tx] = bf2f(B[(k0 + ty) * N + col]);
    __syncthreads();
    for (int k = 0; k < 16; ++k) acc += As[ty][k] * Bs[k][tx];
    __syncthreads();
  }
  if (res) acc += bf2f(res[row * N + col]);
  C[row * N + col] = f2bf(acc);
}
__global__ void k_feat(const u16* qkv, const u16* wphi, float* phi) {
  int idx = blockIdx.x * blockDim.x + threadIdx.x;
  int which = idx / (S * H); int r = idx % (S * H); int s = r / H, h = r % H;
  float q[16];
  for (int d = 0; d < 16; ++d) q[d] = bf2f(qkv[s * 3 * D + which * D + h * DH + d]);
  float p[16]; float mx = -1e30f, mn = 1e30f;
  for (int f = 0; f < 16; ++f) { float a = 0; for (int d = 0; d < 16; ++d) a += q[d] * bf2f(wphi[(h * 16 + d) * 16 + f]); p[f] = a; mx = fmaxf(mx, a); mn = fminf(mn, a); }
  float sp = 0, sn = 0; float ep[16], en[16];
  for (int f = 0; f < 16; ++f) { ep[f] = __expf(p[f] - mx); en[f] = __expf(mn - p[f]); sp += ep[f]; sn += en[f]; }
  float* o = phi + (size_t)idx * 32;
  for (int f = 0; f < 16; ++f) { o[f] = ep[f] / sp; o[16 + f] = en[f] / sn; }
}
__global__ void k_kv(const u16* qkv, const float* phi, float* kv) {
  int h = blockIdx.x; int t = threadIdx.x;
  int f = t / 17, d = t % 17;
  const float* pk = phi + (size_t)S * H * 32;
  float acc = 0;
  for (int s = 0; s < S; ++s) {
    float p = pk[((size_t)s * H + h) * 32 + f];
    float v = d < 16 ? bf2f(qkv[s * 3 * D + 2 * D + h * DH + d]) : 1.f;
    acc += p * v;
  }
  kv[(h * 32 + f) * 17 + d] = acc;
}
__global__ void k_oA(const float* phi, const float* kv, u16* o) {
  int idx = blockIdx.x * blockDim.x + threadIdx.x;
  int d = idx & 15; int h = (idx >> 4) & 31; int s = idx >> 9;
  const float* pq = phi + ((size_t)s * H + h) * 32;
  float num = 0, den = 0;
  for (int f = 0; f < 32; ++f) { num += pq[f] * kv[(h * 32 + f) * 17 + d]; den += pq[f] * kv[(h * 32 + f) * 17 + 16]; }
  o[s * D + h * DH + d] = f2bf(num / (den + 1e-6f));
}
__global__ void k_attnB(const u16* qkv, u16* o) {
  int idx = blockIdx.x * blockDim.x + threadIdx.x;
  int h = idx & 31, s = idx >> 5;
  float q[16];
  for (int d = 0; d < 16; ++d) q[d] = bf2f(qkv[s * 3 * D + h * DH + d]);
  float l[8]; float mx = -1e30f;
  for (int w = 0; w < 8; ++w) {
    int j = s - 2 * w;
    if (j >= 0) { float a = 0; for (int d = 0; d < 16; ++d) a += q[d] * bf2f(qkv[j * 3 * D + D + h * DH + d]); l[w] = a * 0.25f; }
    else l[w] = -1e9f;
    mx = fmaxf(mx, l[w]);
  }
  float sum = 0; for (int w = 0; w < 8; ++w) { l[w] = __expf(l[w] - mx); sum += l[w]; }
  for (int d = 0; d < 16; ++d) {
    float a = 0;
    for (int w = 0; w < 8; ++w) { int j = s - 2 * w; if (j < 0) j = 0; a += l[w] * bf2f(qkv[j * 3 * D + 2 * D + h * DH + d]); }
    o[s * D + h * DH + d] = f2bf(a / sum);
  }
}

extern "C" void kernel_launch(void* const* d_in, const int* in_sizes, int n_in,
                              void* d_out, int out_size, void* d_ws, size_t ws_size,
                              hipStream_t stream) {
  const u16* x0 = (const u16*)d_in[0];
  const u16* Awqkv = (const u16*)d_in[1];
  const u16* Awphi = (const u16*)d_in[2];
  const u16* Awo = (const u16*)d_in[3];
  const u16* Bwqkv = (const u16*)d_in[5];
  const u16* Bwo = (const u16*)d_in[6];
  char* ws = (char*)d_ws;
  u16* xn = (u16*)ws; ws += S * D * 2;
  u16* qkv = (u16*)ws; ws += S * 3 * D * 2;
  u16* o = (u16*)ws; ws += S * D * 2;
  u16* xa = (u16*)ws; ws += S * D * 2;
  u16* xb = (u16*)ws; ws += S * D * 2;
  float* phi = (float*)ws; ws += (size_t)2 * S * H * 32 * 4;
  float* kv = (float*)ws; ws += H * 32 * 17 * 4;
  const u16* xin = x0;
  u16* outs[4] = {xa, xb, xa, (u16*)d_out};
  for (int L = 0; L < 4; ++L) {
    bool isA = (L % 2 == 0);
    k_ln<<<S, 64, 0, stream>>>(xin, xn);
    k_gemm<<<dim3(3 * D / 16, S / 16), 256, 0, stream>>>(xn, isA ? Awqkv : Bwqkv, nullptr, qkv, S, 3 * D, D);
    if (isA) {
      k_feat<<<2 * S * H / 256, 256, 0, stream>>>(qkv, Awphi, phi);
      k_kv<<<H, 544, 0, stream>>>(qkv, phi, kv);
      k_oA<<<S * H * 16 / 256, 256, 0, stream>>>(phi, kv, o);
    } else {
      k_attnB<<<S * H / 256, 256, 0, stream>>>(qkv, o);
    }
    k_gemm<<<dim3(D / 16, S / 16), 256, 0, stream>>>(o, isA ? Awo : Bwo, xin, outs[L], S, D, D);
    xin = outs[L];
  }
}
```

```cpp
#include <hip/hip_runtime.h>
#include <stdint.h>

typedef unsigned short u16;
typedef __bf16 bf16x8 __attribute__((ext_vector_type(8)));
typedef __bf16 bf16x2 __attribute__((ext_vector_type(2)));
typedef short s16x4 __attribute__((ext_vector_type(4)));
typedef float f32x4 __attribute__((ext_vector_type(4)));
typedef float f32x2 __attribute__((ext_vector_type(2)));
typedef unsigned u32x2 __attribute__((ext_vector_type(2)));
typedef unsigned u32x4 __attribute__((ext_vector_type(4)));
#define LAS __attribute__((address_space(3)))
#define GAS __attribute__((address_space(1)))
typedef GAS unsigned long long gu64;
#define RLX_AGENT __ATOMIC_RELAXED, __HIP_MEMORY_SCOPE_AGENT
#define DI __device__ __forceinline__

constexpr int WS_FLAGS = 4096;
constexpr int EDGE_X = 0, EDGE_O = 4, EDGE_KV = 8;
constexpr int WS_XF = 65536;
constexpr int FRAG_BUF = 2 << 20;
constexpr int WS_OF = WS_XF + 4 * FRAG_BUF;
constexpr int WS_XS = WS_OF + 4 * FRAG_BUF;
constexpr int XS_BUF = 128 * 16 * 16 * 8;
constexpr int WS_KV = WS_XS + 4 * XS_BUF;
constexpr int KV_BUF = 16 * 16 * 4 * 1024;
constexpr int WS_KZ = WS_KV + 2 * KV_BUF;
constexpr int KZ_BUF = 16 * 16 * 4 * 64;
constexpr int WS_END = WS_KZ + 2 * KZ_BUF;
constexpr int WS_STAMP = WS_END;

constexpr int LDS_W = 0;
constexpr int LDS_WO = 98304;
constexpr int LDS_BYTES = 163840;
constexpr int AUX_SC1 = 16;
constexpr unsigned SPIN_CAP = 1u << 16;

DI unsigned pk2(float a, float b) { f32x2 v = {a, b}; return __builtin_bit_cast(unsigned, __builtin_convertvector(v, bf16x2)); }
DI float bflo(unsigned u) { return __uint_as_float(u << 16); }
DI float bfhi(unsigned u) { return __uint_as_float(u & 0xffff0000u); }
DI f32x4 mfma32(u32x4 a, u32x4 b, f32x4 c) {
  return __builtin_amdgcn_mfma_f32_16x16x32_bf16(__builtin_bit_cast(bf16x8, a), __builtin_bit_cast(bf16x8, b), c, 0, 0, 0);
}
DI f32x4 mfma16(u32x2 a, u32x2 b, f32x4 c) {
  return __builtin_amdgcn_mfma_f32_16x16x16bf16_1k(__builtin_bit_cast(s16x4, a), __builtin_bit_cast(s16x4, b), c, 0, 0, 0);
}
DI u32x2 tr_read(LAS unsigned char* p) { return __builtin_bit_cast(u32x2, __builtin_amdgcn_ds_read_tr16_b64_v4i16((LAS s16x4*)p)); }
DI u32x4 tr_frag(LAS unsigned char* p, int second) {
  u32x2 a = tr_read(p), b = tr_read(p + second);
  return u32x4{a.x, a.y, b.x, b.y};
}
DI void swap32(unsigned& a, unsigned& b) { auto r = __builtin_amdgcn_permlane32_swap(a, b, false, false); a = r[0]; b = r[1]; }
DI void swap16(unsigned& a, unsigned& b) { auto r = __builtin_amdgcn_permlane16_swap(a, b, false, false); a = r[0]; b = r[1]; }
DI void swap32f(float& a, float& b) { unsigned x = __float_as_uint(a), y = __float_as_uint(b); swap32(x, y); a = __uint_as_float(x); b = __uint_as_float(y); }
DI void swap16f(float& a, float& b) { unsigned x = __float_as_uint(a), y = __float_as_uint(b); swap16(x, y); a = __uint_as_float(x); b = __uint_as_float(y); }
DI void xsum2(float& s1, float& s2) {
  swap32f(s1, s2);
  float t = s1 + s2, u = t;
  swap16f(t, u);
  float a = t + u, b = a;
  swap32f(a, b);
  s1 = a; s2 = b;
}
DI float xmax1(float x) {
  float a = x, b = x;
  swap32f(a, b);
  float y = fmaxf(a, b), z = y;
  swap16f(y, z);
  return fmaxf(y, z);
}
#define BAR() asm volatile("s_waitcnt lgkmcnt(0)\n\ts_barrier" ::: "memory")
#define DRAIN() asm volatile("s_waitcnt vmcnt(0)" ::: "memory")
#define SB() __builtin_amdgcn_sched_barrier(0)

DI void dma16(const void* sbase, unsigned voff, unsigned lds_addr) {
  unsigned keep;
  asm volatile("s_mov_b32 %0, m0\n\ts_mov_b32 m0, %3\n\ts_nop 0\n\tglobal_load_lds_dwordx4 %1, %2\n\ts_mov_b32 m0, %0"
               : "=&s"(keep) : "v"(voff), "s"(sbase), "s"(lds_addr) : "memory");
}

struct Ctx {
  __amdgpu_buffer_rsrc_t rs;
  gu64* flags;
  unsigned long long tag;
  LAS unsigned char* L;
  unsigned ldsbase;
  int lane, wave, c, g, gi, gj;
  unsigned wvoff, wovoff;
};

DI u32x4 ld128(const Ctx& x, int voff, int soff) { return __builtin_amdgcn_raw_buffer_load_b128(x.rs, voff, soff, AUX_SC1); }
DI u32x2 ld64(const Ctx& x, int voff, int soff) { return __builtin_amdgcn_raw_buffer_load_b64(x.rs, voff, soff, AUX_SC1); }
DI void st128(const Ctx& x, u32x4 v, int voff, int soff) { __builtin_amdgcn_raw_buffer_store_b128(v, x.rs, voff, soff, AUX_SC1); }
DI void st64(const Ctx& x, u32x2 v, int voff, int soff) { __builtin_amdgcn_raw_buffer_store_b64(v, x.rs, voff, soff, AUX_SC1); }

#define STAMP(n) do {} while (0)

DI void dma_w(const Ctx& x, const u16* W, int u0, int u1) {
  if (x.wave != 0) {
    const char* src = (const char*)W + (size_t)u0 * 49152;
    unsigned dst = x.ldsbase + LDS_W + (u0 * 3 + x.wave - 1) * 1024;
    for (int u = u0; u < u1; ++u) { dma16(src, x.wvoff, dst); src += 49152; dst += 3072; }
  }
}
DI void dma_wo(const Ctx& x, const u16* Wo) {
  if (x.wave != 0) {
    const char* src = (const char*)Wo + (size_t)(x.wave - 1) * 8192;
    unsigned dst = x.ldsbase + LDS_WO + (x.wave - 1) * 1024;
    for (int n = x.wave - 1; n < 64; n += 3) { dma16(src, x.wovoff, dst); src += 3 * 8192; dst += 3072; }
  }
}

DI void publish(const Ctx& x, int edge, int line, int slot) {
  DRAIN();
  BAR();
  if (threadIdx.x == 64) __hip_atomic_store(x.flags + (edge * 16 + line) * 16 + slot, x.tag, RLX_AGENT);
}
DI void wait_flags(const Ctx& x, int edge, int line, int line2) {
  if (x.wave == 0) {
    int idx = (edge * 16 + line) * 16 + (x.lane & 15);
    if (line2 >= 0 && x.lane >= 16 && x.lane < 24) idx = (edge * 16 + line2) * 16 + (x.lane - 8);
    gu64* p = x.flags + idx;
    for (unsigned spins = 0; spins < SPIN_CAP; ++spins) {
      unsigned long long v = __hip_atomic_load(p, RLX_AGENT);
      if (__all(v == x.tag)) break;
    }
    __builtin_amdgcn_fence(__ATOMIC_ACQUIRE, "wavefront");
  }
  BAR();
}

DI void store_x(const Ctx& x, int L, const f32x4 (&r)[2][2], int tau0, int sblk) {
#pragma unroll
  for (int t = 0; t < 2; ++t) {
    u32x4 f = {pk2(r[t][0].x, r[t][0].y), pk2(r[t][0].z, r[t][0].w), pk2(r[t][1].x, r[t][1].y), pk2(r[t][1].z, r[t][1].w)};
    st128(x, f, x.lane * 16, WS_XF + L * FRAG_BUF + ((tau0 + t) * 16 + sblk) * 1024);
    float s1 = 0.f, s2 = 0.f;
#pragma unroll
    for (int a = 0; a < 2; ++a)
#pragma unroll
      for (int e = 0; e < 4; ++e) { float v = r[t][a][e]; s1 += v; s2 = fmaf(v, v, s2); }
    xsum2(s1, s2);
    if (x.g == 0) st64(x, u32x2{__float_as_uint(s1), __float_as_uint(s2)}, x.c * 8, WS_XS + L * XS_BUF + ((tau0 + t) * 16 + sblk) * 128);
  }
}

DI void finish_stats(const u32x2 (&p)[4], float& mean, float& rstd) {
  float s1 = 0.f, s2 = 0.f;
#pragma unroll
  for (int u = 0; u < 4; ++u) { s1 += __uint_as_float(p[u].x); s2 += __uint_as_float(p[u].y); }
  xsum2(s1, s2);
  mean = s1 * (1.f / 512.f);
  float var = fmaxf(s2 * (1.f / 512.f) - mean * mean, 0.f);
  rstd = __builtin_amdgcn_rsqf(var + 1e-5f);
}
DI u32x2 ln_pack(f32x4 a, f32x4 cs, float mean, float rstd) {
  float v0 = fmaf(-mean, cs.x, a.x) * rstd, v1 = fmaf(-mean, cs.y, a.y) * rstd;
  float v2 = fmaf(-mean, cs.z, a.z) * rstd, v3 = fmaf(-mean, cs.w, a.w) * rstd;
  return u32x2{pk2(v0, v1), pk2(v2, v3)};
}

DI void feature_map(const u32x2 (&in)[2], const u32x2 (&wp)[4], unsigned (&o)[8]) {
  f32x4 p[4];
  const f32x4 z = {0.f, 0.f, 0.f, 0.f};
#pragma unroll
  for (int i = 0; i < 4; ++i) p[i] = mfma16(wp[i], in[i >> 1], z);
  float v[4][4];
#pragma unroll
  for (int i = 0; i < 4; ++i)
#pragma unroll
    for (int r = 0; r < 4; ++r) v[i][r] = p[i][r];
#pragma unroll
  for (int r = 0; r < 4; ++r) { swap32f(v[0][r], v[2][r]); swap32f(v[1][r], v[3][r]); }
#pragma unroll
  for (int r = 0; r < 4; ++r) { swap16f(v[0][r], v[1][r]); swap16f(v[2][r], v[3][r]); }
  float m = v[0][0];
#pragma unroll
  for (int i = 0; i < 4; ++i)
#pragma unroll
    for (int r = 0; r < 4; ++r) m = fmaxf(m, v[i][r]);
  const float LOG2E = 1.4426950408889634f;
  const float mm = -m * LOG2E;
  float s = 0.f;
#pragma unroll
  for (int i = 0; i < 4; ++i)
#pragma unroll
    for (int r = 0; r < 4; ++r) { v[i][r] = __builtin_amdgcn_exp2f(fmaf(v[i][r], LOG2E, mm)); s += v[i][r]; }
  const float rs = __builtin_amdgcn_rcpf(s);
#pragma unroll
  for (int i = 0; i < 4; ++i) { o[2 * i] = pk2(v[i][0] * rs, v[i][1] * rs); o[2 * i + 1] = pk2(v[i][2] * rs, v[i][3] * rs); }
}

template <bool IS_B>
DI void qkv_phase(const Ctx& x, int L, const u32x2 (&wp)[4], const u16* Wo, const u16* Wnext, unsigned char* wsp) {
  const int lane = x.lane, w = x.wave, c = x.c, g = x.g;
  const int q = c >> 2, p = c & 3;
  LAS unsigned char* we = x.L + LDS_W + (4 * g + q) * 192 + p * 8 + (g & 1) * 32;
  LAS unsigned char* wo_ = x.L + LDS_W + (4 * g + q) * 192 + p * 8 - (g & 1) * 32;
  f32x4 cs[6];
  {
    const u32x4 ones = {0x3f803f80u, 0x3f803f80u, 0x3f803f80u, 0x3f803f80u};
#pragma unroll
    for (int ct = 0; ct < 6; ++ct) cs[ct] = f32x4{0.f, 0.f, 0.f, 0.f};
#pragma unroll 2
    for (int s = 0; s < 16; ++s) {
#pragma unroll
      for (int ct = 0; ct < 6; ++ct) cs[ct] = mfma32(tr_frag(((ct & 1) ? wo_ : we) + s * 6144 + ct * 32, 3072), ones, cs[ct]);
    }
  }
  STAMP(4 + L * 12 + 0);
  wait_flags(x, EDGE_X + L, x.gi, (IS_B && x.gi > 0) ? x.gi - 1 : -1);
  STAMP(4 + L * 12 + 1);
  const int tau0 = 8 * x.gi + 2 * w;
  u32x4 xf[2][16];
  const int xbase = WS_XF + L * FRAG_BUF + tau0 * 16384;
#pragma unroll
  for (int s = 0; s < 16; ++s)
#pragma unroll
    for (int t = 0; t < 2; ++t) xf[t][s] = ld128(x, lane * 16, xbase + t * 16384 + s * 1024);
  u32x2 sp[2][4];
#pragma unroll
  for (int t = 0; t < 2; ++t)
#pragma unroll
    for (int u = 0; u < 4; ++u) sp[t][u] = ld64(x, (4 * g + u) * 128 + c * 8, WS_XS + L * XS_BUF + (tau0 + t) * 2048);
  u32x4 hx[4];
  u32x2 hsp[4];
  const bool has_halo = IS_B && x.gi > 0;
  if (IS_B) {
    const int gih = x.gi > 0 ? x.gi : 1;
    const int ht = 8 * gih - 2 + (c >> 3), hc = 8 + (c & 7);
    if (has_halo) {
#pragma unroll
      for (int s = 0; s < 4; ++s) hx[s] = ld128(x, ht * 16384 + (g * 16 + hc) * 16, WS_XF + L * FRAG_BUF + (4 * w + s) * 1024);
#pragma unroll
      for (int u = 0; u < 4; ++u) hsp[u] = ld64(x, ht * 2048 + (4 * g + u) * 128 + hc * 8, WS_XS + L * XS_BUF);
    } else {
#pragma unroll
      for (int s = 0; s < 4; ++s) hx[s] = u32x4{0u, 0u, 0u, 0u};
#pragma unroll
      for (int u = 0; u < 4; ++u) hsp[u] = u32x2{0u, 0u};
    }
  }
  SB();
  f32x4 acc[2][6];
  f32x4 hacc[4];
#pragma unroll
  for (int t = 0; t < 2; ++t)
#pragma unroll
    for (int ct = 0; ct < 6; ++ct) acc[t][ct] = f32x4{0.f, 0.f, 0.f, 0.f};
#pragma unroll
  for (int i = 0; i < 4; ++i) hacc[i] = f32x4{0.f, 0.f, 0.f, 0.f};
#pragma unroll
  for (int s = 0; s < 16; ++s) {
    u32x4 wf[6];
#pragma unroll
    for (int ct = 0; ct < 6; ++ct) wf[ct] = tr_frag(((ct & 1) ? wo_ : we) + s * 6144 + ct * 32, 3072);
#pragma unroll
    for (int ct = 0; ct < 6; ++ct)
#pragma unroll
      for (int t = 0; t < 2; ++t) acc[t][ct] = mfma32(wf[ct], xf[t][s], acc[t][ct]);
    if (IS_B) {
      if (w == (s >> 2)) {
#pragma unroll
        for (int i = 0; i < 4; ++i) hacc[i] = mfma32(wf[2 + i], hx[s & 3], hacc[i]);
      }
    }
  }
  float mean[2], rstd[2];
#pragma unroll
  for (int t = 0; t < 2; ++t) finish_stats(sp[t], mean[t], rstd[t]);
  STAMP(4 + L * 12 + 2);
  BAR();
  dma_wo(x, Wo);
  LAS unsigned char* S = x.L + LDS_W;
  const int obase = WS_OF + L * FRAG_BUF + tau0 * 16384 + x.gj * 1024;
  if (!IS_B) {
    constexpr int PHI = 0, VIM = 16384, KVOP = 24576;
#pragma unroll
    for (int t = 0; t < 2; ++t) {
      const int row = (2 * w + t) * 16 + c;
      u32x2 kn[2];
#pragma unroll
      for (int h = 0; h < 2; ++h) {
        kn[h] = ln_pack(acc[t][2 + h], cs[2 + h], mean[t], rstd[t]);
        *(LAS u32x2*)(S + VIM + h * 4096 + row * 32 + g * 8) = ln_pack(acc[t][4 + h], cs[4 + h], mean[t], rstd[t]);
      }
      unsigned ph[8];
      feature_map(kn, wp, ph);
      *(LAS u32x4*)(S + PHI + g * 4096 + row * 32) = u32x4{ph[0], ph[1], ph[2], ph[3]};
      *(LAS u32x4*)(S + PHI + g * 4096 + row * 32 + 16) = u32x4{ph[4], ph[5], ph[6], ph[7]};
    }
    BAR();
    {
      const u32x4 ones = {0x3f803f80u, 0x3f803f80u, 0x3f803f80u, 0x3f803f80u};
      f32x4 kv = {0.f, 0.f, 0.f, 0.f}, zz = {0.f, 0.f, 0.f, 0.f};
      LAS unsigned char* pa = S + PHI + w * 4096 + (4 * g + q) * 32 + p * 8;
      LAS unsigned char* pb = S + VIM + (w >> 1) * 4096 + (4 * g + q) * 32 + p * 8;
#pragma unroll
      for (int u = 0; u < 4; ++u) {
        u32x4 a = tr_frag(pa + u * 1024, 512), b = tr_frag(pb + u * 1024, 512);
        kv = mfma32(a, b, kv);
        zz = mfma32(a, ones, zz);
      }
      const int e = L >> 1;
      st128(x, __builtin_bit_cast(u32x4, kv), lane * 16, WS_KV + e * KV_BUF + ((x.gj * 16 + x.gi) * 4 + w) * 1024);
      if (c == 0) st128(x, __builtin_bit_cast(u32x4, zz), g * 16, WS_KZ + e * KZ_BUF + ((x.gj * 16 + x.gi) * 4 + w) * 64);
    }
    STAMP(4 + L * 12 + 3);
    publish(x, EDGE_KV + (L >> 1), x.gj, x.gi);
    u32x4 pq[2][2];
#pragma unroll
    for (int t = 0; t < 2; ++t) {
      u32x2 qn[2];
#pragma unroll
      for (int h = 0; h < 2; ++h) qn[h] = ln_pack(acc[t][h], cs[h], mean[t], rstd[t]);
      unsigned ph[8];
      feature_map(qn, wp, ph);
#pragma unroll
      for (int r = 0; r < 2; ++r) { swap16(ph[r], ph[2 + r]); swap16(ph[4 + r], ph[6 + r]); }
#pragma unroll
      for (int r = 0; r < 2; ++r) { swap32(ph[r], ph[4 + r]); swap32(ph[2 + r], ph[6 + r]); }
      pq[t][0] = u32x4{ph[0], ph[1], ph[2], ph[3]};
      pq[t][1] = u32x4{ph[4], ph[5], ph[6], ph[7]};
    }
    STAMP(4 + L * 12 + 4);
    wait_flags(x, EDGE_KV + (L >> 1), x.gj, -1);
    STAMP(4 + L * 12 + 5);
    {
      const int e = L >> 1;
      u32x4 a[16], b[16];
#pragma unroll
      for (int i = 0; i < 16; ++i) a[i] = ld128(x, lane * 16, WS_KV + e * KV_BUF + ((x.gj * 16 + i) * 4 + w) * 1024);
#pragma unroll
      for (int i = 0; i < 16; ++i) b[i] = ld128(x, g * 16, WS_KZ + e * KZ_BUF + ((x.gj * 16 + i) * 4 + w) * 64);
      SB();
      f32x4 kv = __builtin_bit_cast(f32x4, a[0]), zz = __builtin_bit_cast(f32x4, b[0]);
#pragma unroll
      for (int i = 1; i < 16; ++i) kv += __builtin_bit_cast(f32x4, a[i]);
#pragma unroll
      for (int i = 1; i < 16; ++i) zz += __builtin_bit_cast(f32x4, b[i]);
      *(LAS u32x2*)(S + KVOP + (w >> 1) * 2048 + lane * 16 + (w & 1) * 8) = u32x2{pk2(kv.x, kv.y), pk2(kv.z, kv.w)};
      *(LAS u32x2*)(S + KVOP + (w >> 1) * 2048 + 1024 + lane * 16 + (w & 1) * 8) = u32x2{pk2(zz.x, zz.y), pk2(zz.z, zz.w)};
    }
    BAR();
    {
      u32x4 kvop[2], zop[2];
#pragma unroll
      for (int h = 0; h < 2; ++h) {
        kvop[h] = *(LAS u32x4*)(S + KVOP + h * 2048 + lane * 16);
        zop[h] = *(LAS u32x4*)(S + KVOP + h * 2048 + 1024 + lane * 16);
      }
      const f32x4 z = {0.f, 0.f, 0.f, 0.f};
#pragma unroll
      for (int t = 0; t < 2; ++t) {
        unsigned o[4];
#pragma unroll
        for (int h = 0; h < 2; ++h) {
          f32x4 num = mfma32(kvop[h], pq[t][h], z), den = mfma32(zop[h], pq[t][h], z);
          const float r = __builtin_amdgcn_rcpf(den.x + 1e-6f);
          o[2 * h] = pk2(num.x * r, num.y * r); o[2 * h + 1] = pk2(num.z * r, num.w * r);
        }
        st128(x, u32x4{o[0], o[1], o[2], o[3]}, lane * 16, obase + t * 16384);
      }
    }
  } else {
    constexpr int KIM = 0, VIM = 10240, HP = 20480;
    u32x2 qn[2][2], kn[2][2];
#pragma unroll
    for (int i = 0; i < 4; ++i) *(LAS f32x4*)(S + HP + (w * 4 + i) * 1024 + lane * 16) = hacc[i];
#pragma unroll
    for (int t = 0; t < 2; ++t) {
      const int slot = 2 + 2 * w + t;
#pragma unroll
      for (int h = 0; h < 2; ++h) {
        kn[t][h] = ln_pack(acc[t][2 + h], cs[2 + h], mean[t], rstd[t]);
        *(LAS u32x2*)(S + KIM + (slot * 2 + h) * 512 + lane * 8) = kn[t][h];
        *(LAS u32x2*)(S + VIM + (slot * 2 + h) * 512 + c * 32 + g * 8) = ln_pack(acc[t][4 + h], cs[4 + h], mean[t], rstd[t]);
        qn[t][h] = ln_pack(acc[t][h], cs[h], mean[t], rstd[t]);
      }
    }
    float hm, hr;
    finish_stats(hsp, hm, hr);
    BAR();
    {
      f32x4 a = *(LAS f32x4*)(S + HP + w * 1024 + lane * 16);
#pragma unroll
      for (int i = 1; i < 4; ++i) a += *(LAS f32x4*)(S + HP + (i * 4 + w) * 1024 + lane * 16);
      const f32x4 csw = w == 0 ? cs[2] : w == 1 ? cs[3] : w == 2 ? cs[4] : cs[5];
      u32x2 v = ln_pack(a, csw, hm, hr);
      if (!has_halo) v = u32x2{0u, 0u};
      const int slot = c >> 3, hc = 8 + (c & 7), h = w & 1;
      const u32x2 zero = {0u, 0u};
      if (w < 2) {
        *(LAS u32x2*)(S + KIM + (slot * 2 + h) * 512 + (g * 16 + hc) * 8) = v;
        *(LAS u32x2*)(S + KIM + (slot * 2 + h) * 512 + (g * 16 + hc - 8) * 8) = zero;
      } else {
        *(LAS u32x2*)(S + VIM + (slot * 2 + h) * 512 + hc * 32 + g * 8) = v;
        *(LAS u32x2*)(S + VIM + (slot * 2 + h) * 512 + (hc - 8) * 32 + g * 8) = zero;
      }
    }
    BAR();
    {
      const float NEG = -1e30f;
      f32x4 bo, bp;
      const bool noprev = (w == 0) && !has_halo;
#pragma unroll
      for (int r = 0; r < 4; ++r) {
        const int key = 4 * g + r;
        bo[r] = (c - key >= 0 && c - key <= 7) ? 0.f : NEG;
        bp[r] = (key >= c + 9 && !noprev) ? 0.f : NEG;
      }
      const u32x4 ones = {0x3f803f80u, 0x3f803f80u, 0x3f803f80u, 0x3f803f80u};
      const f32x4 z = {0.f, 0.f, 0.f, 0.f};
      const float SC = 0.25f * 1.4426950408889634f;
#pragma unroll
      for (int t = 0; t < 2; ++t) {
        const int slot = 2 + 2 * w + t;
        unsigned o[4];
#pragma unroll
        for (int h = 0; h < 2; ++h) {
          const u32x2 kp = *(LAS u32x2*)(S + KIM + ((slot - 2) * 2 + h) * 512 + lane * 8);
          const u32x2 vo = tr_read(S + VIM + (slot * 2 + h) * 512 + (4 * g + q) * 32 + p * 8);
          const u32x2 vp = tr_read(S + VIM + ((slot - 2) * 2 + h) * 512 + (4 * g + q) * 32 + p * 8);
          f32x4 so = mfma16(kn[t][h], qn[t][h], bo), sp_ = mfma16(kp, qn[t][h], bp);
          float m = fmaxf(fmaxf(fmaxf(so.x, so.y), fmaxf(so.z, so.w)), fmaxf(fmaxf(sp_.x, sp_.y), fmaxf(sp_.z, sp_.w)));
          m = xmax1(m);
          const float mm = -m * SC;
          float e[8];
#pragma unroll
          for (int r = 0; r < 4; ++r) { e[r] = __builtin_amdgcn_exp2f(fmaf(so[r], SC, mm)); e[4 + r] = __builtin_amdgcn_exp2f(fmaf(sp_[r], SC, mm)); }
          const u32x4 P = {pk2(e[0], e[1]), pk2(e[2], e[3]), pk2(e[4], e[5]), pk2(e[6], e[7])};
          f32x4 num = mfma32(u32x4{vo.x, vo.y, vp.x, vp.y}, P, z), den = mfma32(ones, P, z);
          const float r = __builtin_amdgcn_rcpf(den.x);
          o[2 * h] = pk2(num.x * r, num.y * r); o[2 * h + 1] = pk2(num.z * r, num.w * r);
        }
        st128(x, u32x4{o[0], o[1], o[2], o[3]}, lane * 16, obase + t * 16384);
      }
    }
  }
  STAMP(4 + L * 12 + 6);
  publish(x, EDGE_O + L, x.gi, x.gj);
  if (Wnext) dma_w(x, Wnext, 0, 16);
}

DI void oproj_phase(const Ctx& x, int L, f32x4 (&res)[2][2], const u16* Wnext, u16* out, unsigned char* wsp) {
  const int lane = x.lane, w = x.wave, c = x.c, g = x.g;
  const int q = c >> 2, p = c & 3;
  const int tm = w & 1, kh = w >> 1;
  const int tau0 = 8 * x.gi + 4 * (x.gj >> 3) + 2 * tm;
  STAMP(4 + L * 12 + 7);
  wait_flags(x, EDGE_O + L, x.gi, -1);
  STAMP(4 + L * 12 + 8);
  u32x4 of[2][8];
  const int obase = WS_OF + L * FRAG_BUF + tau0 * 16384 + kh * 8192;
#pragma unroll
  for (int s = 0; s < 8; ++s)
#pragma unroll
    for (int t = 0; t < 2; ++t) of[t][s] = ld128(x, lane * 16, obase + t * 16384 + s * 1024);
  SB();
  if (Wnext) dma_w(x, Wnext, 16, 32);
  SB();
  f32x4 acc[2][4];
#pragma unroll
  for (int t = 0; t < 2; ++t)
#pragma unroll
    for (int ct = 0; ct < 4; ++ct) acc[t][ct] = f32x4{0.f, 0.f, 0.f, 0.f};
  const int sw = ((g & 1) << 1) | (q >> 1);
  LAS unsigned char* wb = x.L + LDS_WO + kh * 32768 + (4 * g + q) * 128 + p * 8;
#pragma unroll
  for (int s = 0; s < 8; ++s) {
    u32x4 wf[4];
#pragma unroll
    for (int ct = 0; ct < 4; ++ct) wf[ct] = tr_frag(wb + ((ct ^ sw) << 5) + s * 4096, 2048);
#pragma unroll
    for (int ct = 0; ct < 4; ++ct)
#pragma unroll
      for (int t = 0; t < 2; ++t) acc[t][ct] = mfma32(wf[ct], of[t][s], acc[t][ct]);
  }
  STAMP(4 + L * 12 + 9);
  BAR();
  LAS unsigned char* S = x.L + LDS_WO;
#pragma unroll
  for (int t = 0; t < 2; ++t)
#pragma unroll
    for (int a = 0; a < 2; ++a) *(LAS f32x4*)(S + (w * 4 + t * 2 + a) * 1024 + lane * 16) = kh ? acc[t][a] : acc[t][2 + a];
  BAR();
#pragma unroll
  for (int t = 0; t < 2; ++t)
#pragma unroll
    for (int a = 0; a < 2; ++a) {
      const f32x4 other = *(LAS f32x4*)(S + ((w ^ 2) * 4 + t * 2 + a) * 1024 + lane * 16);
      const f32x4 mine = kh ? acc[t][2 + a] : acc[t][a];
      res[t][a] += mine + other;
    }
  const int sblk = 2 * (x.gj & 7) + kh;
  if (L < 3) {
    store_x(x, L + 1, res, tau0, sblk);
    STAMP(4 + L * 12 + 10);
    publish(x, EDGE_X + L + 1, x.gi, x.gj);
  } else {
#pragma unroll
    for (int t = 0; t < 2; ++t) {
      const int tok = 32 * ((tau0 + t) >> 1) + 2 * c + ((tau0 + t) & 1);
#pragma unroll
      for (int a = 0; a < 2; ++a)
        *(GAS u32x2*)(out + tok * 512 + sblk * 32 + a * 16 + 4 * g) = u32x2{pk2(res[t][a].x, res[t][a].y), pk2(res[t][a].z, res[t][a].w)};
    }
    STAMP(4 + L * 12 + 10);
  }
}

__global__ void __launch_bounds__(256, 1)
fused_fwd(const u16* __restrict__ x0, const u16* __restrict__ wA, const u16* __restrict__ wphi, const u16* __restrict__ woA,
          const u16* __restrict__ wB, const u16* __restrict__ woB, u16* __restrict__ out, unsigned char* __restrict__ wsp) {
  extern __shared__ __attribute__((aligned(16))) unsigned char smem[];
  Ctx x;
  x.L = (LAS unsigned char*)smem;
  x.ldsbase = (unsigned)(uintptr_t)x.L;
  x.lane = threadIdx.x & 63;
  x.wave = __builtin_amdgcn_readfirstlane(threadIdx.x >> 6);
  x.c = x.lane & 15; x.g = x.lane >> 4;
  const int b = blockIdx.x;
  x.gi = (b & 7) * 2 + (b >> 7);
  x.gj = (b >> 3) & 15;
  x.rs = __builtin_amdgcn_make_buffer_rsrc((void*)wsp, 0, WS_END, 0x00020000);
  x.flags = (gu64*)(wsp + WS_FLAGS);
  STAMP(0);
  const unsigned long long epoch = __hip_atomic_load((gu64*)wsp, RLX_AGENT);
  const int lane = x.lane, w = x.wave, c = x.c, g = x.g;
  {
    const int ci = (w > 0 ? w - 1 : 0) * 64 + lane, row = ci / 12, pc = ci % 12, lc = pc ^ (((row >> 2) & 1) << 1);
    x.wvoff = row * 3072 + (lc >> 2) * 1024 + x.gj * 64 + (lc & 3) * 16;
    const int r8 = lane >> 3, p8 = lane & 7, lu = (p8 >> 1) ^ ((r8 >> 1) & 3);
    x.wovoff = r8 * 1024 + (x.gj & 7) * 128 + ((lu << 1) | (p8 & 1)) * 16;
  }
  f32x4 res[2][2];
  const int tau0 = 8 * x.gi + 4 * (x.gj >> 3) + 2 * (w & 1), sblk = 2 * (x.gj & 7) + (w >> 1);
#pragma unroll
  for (int t = 0; t < 2; ++t) {
    const int tok = 32 * ((tau0 + t) >> 1) + 2 * c + ((tau0 + t) & 1);
#pragma unroll
    for (int a = 0; a < 2; ++a) {
      const u32x2 v = *(const GAS u32x2*)(x0 + tok * 512 + sblk * 32 + a * 16 + 4 * g);
      res[t][a] = f32x4{bflo(v.x), bfhi(v.x), bflo(v.y), bfhi(v.y)};
    }
  }
  u32x2 wp[4];
#pragma unroll
  for (int h = 0; h < 2; ++h) {
    const GAS u16* pw = (const GAS u16*)wphi + ((2 * x.gj + h) * 16 + 4 * g) * 16 + c;
    const unsigned a0 = pw[0], a1 = pw[16], a2 = pw[32], a3 = pw[48];
    wp[2 * h] = u32x2{a0 | (a1 << 16), a2 | (a3 << 16)};
    wp[2 * h + 1] = u32x2{wp[2 * h].x ^ 0x80008000u, wp[2 * h].y ^ 0x80008000u};
  }
  x.tag = __builtin_amdgcn_readfirstlane((unsigned)epoch) | ((unsigned long long)__builtin_amdgcn_readfirstlane((unsigned)(epoch >> 32)) << 32);
  x.tag += 1;
  store_x(x, 0, res, tau0, sblk);
  STAMP(1);
  publish(x, EDGE_X + 0, x.gi, x.gj);
  dma_w(x, wA, 0, 32);
  DRAIN();
  BAR();
  STAMP(2);
  for (int it = 0; it < 2; ++it) {
    qkv_phase<false>(x, 2 * it, wp, woA, wB, wsp);
    oproj_phase(x, 2 * it, res, wB, out, wsp);
    if (it == 0 && b == 0 && threadIdx.x == 0) __hip_atomic_store((gu64*)wsp, x.tag, RLX_AGENT);
    qkv_phase<true>(x, 2 * it + 1, wp, woB, it == 0 ? wA : nullptr, wsp);
    oproj_phase(x, 2 * it + 1, res, it == 0 ? wA : nullptr, out, wsp);
  }
  STAMP(3);
}

extern "C" void kernel_launch(void* const* d_in, const int* in_sizes, int n_in, void* d_out, int out_size, void* d_ws, size_t ws_size,
                              hipStream_t stream) {
  (void)in_sizes; (void)n_in; (void)out_size; (void)ws_size;
  hipFuncSetAttribute((const void*)fused_fwd, hipFuncAttributeMaxDynamicSharedMemorySize, LDS_BYTES);
  fused_fwd<<<256, 256, LDS_BYTES, stream>>>((const u16*)d_in[0], (const u16*)d_in[1], (const u16*)d_in[2], (const u16*)d_in[3],
                                             (const u16*)d_in[5], (const u16*)d_in[6], (u16*)d_out, (unsigned char*)d_ws);
}
```

```cpp
#include <hip/hip_runtime.h>
#include <stdint.h>

typedef unsigned short u16;
typedef __bf16 bf16x8 __attribute__((ext_vector_type(8)));
typedef __bf16 bf16x2 __attribute__((ext_vector_type(2)));
typedef short s16x4 __attribute__((ext_vector_type(4)));
typedef float f32x4 __attribute__((ext_vector_type(4)));
typedef float f32x2 __attribute__((ext_vector_type(2)));
typedef unsigned u32x2 __attribute__((ext_vector_type(2)));
typedef unsigned u32x4 __attribute__((ext_vector_type(4)));
#define LAS __attribute__((address_space(3)))
#define GAS __attribute__((address_space(1)))
typedef GAS unsigned long long gu64;
#define RLX_AGENT __ATOMIC_RELAXED, __HIP_MEMORY_SCOPE_AGENT
#define DI __device__ __forceinline__

constexpr int WS_FLAGS = 4096;
constexpr int EDGE_X = 0, EDGE_O = 4, EDGE_KV = 8;
constexpr int WS_XF = 65536;
constexpr int FRAG_BUF = 2 << 20;
constexpr int WS_OF = WS_XF + 4 * FRAG_BUF;
constexpr int WS_XS = WS_OF + 4 * FRAG_BUF;
constexpr int XS_BUF = 128 * 16 * 16 * 8;
constexpr int WS_KV = WS_XS + 4 * XS_BUF;
constexpr int KV_BUF = 16 * 16 * 4 * 1024;
constexpr int WS_KZ = WS_KV + 2 * KV_BUF;
constexpr int KZ_BUF = 16 * 16 * 4 * 64;
constexpr int WS_CS = WS_KZ + 2 * KZ_BUF;
constexpr int WS_END = WS_CS + 256 * 1024;
constexpr int WS_STAMP = WS_END;

constexpr int LDS_W = 0;
constexpr int LDS_F = 98304;
constexpr int LDS_BYTES = 163840;
constexpr int AUX_SC1 = 16;
#define MAP_NJ 8
constexpr unsigned SPIN_CAP = 1u << 16;

DI unsigned pk2(float a, float b) { f32x2 v = {a, b}; return __builtin_bit_cast(unsigned, __builtin_convertvector(v, bf16x2)); }
DI float bflo(unsigned u) { return __uint_as_float(u << 16); }
DI float bfhi(unsigned u) { return __uint_as_float(u & 0xffff0000u); }
DI f32x4 mfma32(u32x4 a, u32x4 b, f32x4 c) {
  return __builtin_amdgcn_mfma_f32_16x16x32_bf16(__builtin_bit_cast(bf16x8, a), __builtin_bit_cast(bf16x8, b), c, 0, 0, 0);
}
DI f32x4 mfma16(u32x2 a, u32x2 b, f32x4 c) {
  return __builtin_amdgcn_mfma_f32_16x16x16bf16_1k(__builtin_bit_cast(s16x4, a), __builtin_bit_cast(s16x4, b), c, 0, 0, 0);
}
DI u32x2 tr_read(LAS unsigned char* p) { return __builtin_bit_cast(u32x2, __builtin_amdgcn_ds_read_tr16_b64_v4i16((LAS s16x4*)p)); }
DI u32x4 tr_frag(LAS unsigned char* p, int second) {
  u32x2 a = tr_read(p), b = tr_read(p + second);
  return u32x4{a.x, a.y, b.x, b.y};
}
DI void swap32(unsigned& a, unsigned& b) { auto r = __builtin_amdgcn_permlane32_swap(a, b, false, false); a = r[0]; b = r[1]; }
DI void swap16(unsigned& a, unsigned& b) { auto r = __builtin_amdgcn_permlane16_swap(a, b, false, false); a = r[0]; b = r[1]; }
DI void swap32f(float& a, float& b) { unsigned x = __float_as_uint(a), y = __float_as_uint(b); swap32(x, y); a = __uint_as_float(x); b = __uint_as_float(y); }
DI void swap16f(float& a, float& b) { unsigned x = __float_as_uint(a), y = __float_as_uint(b); swap16(x, y); a = __uint_as_float(x); b = __uint_as_float(y); }
DI void xsum2(float& s1, float& s2) {
  swap32f(s1, s2);
  float t = s1 + s2, u = t;
  swap16f(t, u);
  float a = t + u, b = a;
  swap32f(a, b);
  s1 = a; s2 = b;
}
DI float xmax1(float x) {
  float a = x, b = x;
  swap32f(a, b);
  float y = fmaxf(a, b), z = y;
  swap16f(y, z);
  return fmaxf(y, z);
}
#define BAR() asm volatile("s_waitcnt lgkmcnt(0)\n\ts_barrier" ::: "memory")
#define DRAIN() asm volatile("s_waitcnt vmcnt(0)" ::: "memory")
#define SB() __builtin_amdgcn_sched_barrier(0)
#define OPAQUE_V(x) asm volatile("" : "+v"(x))
#define OPAQUE_S(x) asm volatile("" : "+s"(x))

DI void dma16(const void* sbase, unsigned voff, unsigned lds_addr) {
  unsigned keep;
  asm volatile("s_mov_b32 %0, m0\n\ts_mov_b32 m0, %3\n\ts_nop 2\n\tglobal_load_lds_dwordx4 %1, %2\n\ts_mov_b32 m0, %0"
               : "=&s"(keep) : "v"(voff), "s"(sbase), "s"(lds_addr) : "memory");
}

struct Ctx {
  __amdgpu_buffer_rsrc_t rs;
  gu64* flags;
  unsigned long long tag;
  int lane, wave, gi, gj;
  unsigned wv0, wv1, wv2, wovoff;
};

DI u32x4 ld128(const Ctx& x, int voff, int soff) { return __builtin_amdgcn_raw_buffer_load_b128(x.rs, voff, soff, AUX_SC1); }
DI u32x2 ld64(const Ctx& x, int voff, int soff) { return __builtin_amdgcn_raw_buffer_load_b64(x.rs, voff, soff, AUX_SC1); }
DI void st128(const Ctx& x, u32x4 v, int voff, int soff) { __builtin_amdgcn_raw_buffer_store_b128(v, x.rs, voff + soff, 0, AUX_SC1); }
DI void st64(const Ctx& x, u32x2 v, int voff, int soff) { __builtin_amdgcn_raw_buffer_store_b64(v, x.rs, voff, soff, AUX_SC1); }
DI LAS unsigned char* lds_at(int off) { return (LAS unsigned char*)(uintptr_t)(unsigned)off; }

#define STAMP(n) do {} while (0)
#define CSTAMP(n) do {} while (0)

DI void dma_wblk(const Ctx& x, const u16* W, int s) {
  const char* src = (const char*)W + (size_t)s * 98304;
  const unsigned dst = LDS_W + s * 6144;
  dma16(src, x.wv0, dst); dma16(src, x.wv1, dst + 1024); dma16(src, x.wv2, dst + 2048);
  src += 49152;
  dma16(src, x.wv0, dst + 3072); dma16(src, x.wv1, dst + 4096); dma16(src, x.wv2, dst + 5120);
}
DI void relayout_wblk(const Ctx& x, int s) {
  int lane = x.lane;
  OPAQUE_V(lane);
  const int c = lane & 15, g = lane >> 4;
  const int base = LDS_W + s * 6144;
  const int a = base + (4 * g + (c >> 2)) * 192 + (c & 3) * 8;
  u32x4 f[6];
#pragma unroll
  for (int ct = 0; ct < 6; ++ct) f[ct] = tr_frag(lds_at(a + ((ct & 1) ? -(g & 1) * 32 : (g & 1) * 32)) + ct * 32, 3072);
  asm volatile("s_waitcnt lgkmcnt(0)" : "+v"(f[0]), "+v"(f[1]), "+v"(f[2]), "+v"(f[3]), "+v"(f[4]), "+v"(f[5]) :: "memory");
#pragma unroll
  for (int ct = 0; ct < 6; ++ct) *(LAS u32x4*)(lds_at(base + lane * 16) + ct * 1024) = f[ct];
}
DI void dma_wo(const Ctx& x, const u16* Wo) {
  if (x.wave != 0) {
#pragma unroll 1
    for (int n = x.wave - 1; n < 64; n += 7) dma16((const char*)Wo + (size_t)n * 8192, x.wovoff, LDS_F + n * 1024);
  }
}

DI void set_flag(const Ctx& x, int edge, int line, int slot) {
  if (threadIdx.x == 0) __hip_atomic_store(x.flags + (edge * 16 + line) * 16 + slot, x.tag, RLX_AGENT);
}
DI void wait_flags(const Ctx& x, int edge, int line, int line2) {
  if (x.wave == 0) {
    int idx = (edge * 16 + line) * 16 + (x.lane & 15);
    if (line2 >= 0 && x.lane >= 16 && x.lane < 24) idx = (edge * 16 + line2) * 16 + (x.lane - 8);
    gu64* p = x.flags + idx;
    for (unsigned spins = 0; spins < SPIN_CAP; ++spins) {
      unsigned long long v = __hip_atomic_load(p, RLX_AGENT);
      if (__all(v == x.tag)) break;
    }
    __builtin_amdgcn_fence(__ATOMIC_ACQUIRE, "wavefront");
  }
  DRAIN();
  BAR();
}

DI void store_x(const Ctx& x, int L, const f32x4 (&r)[2], int tau, int sblk) {
  u32x4 f = {pk2(r[0].x, r[0].y), pk2(r[0].z, r[0].w), pk2(r[1].x, r[1].y), pk2(r[1].z, r[1].w)};
  st128(x, f, x.lane * 16, WS_XF + L * FRAG_BUF + (tau * 16 + sblk) * 1024);
  float s1 = 0.f, s2 = 0.f;
#pragma unroll
  for (int a = 0; a < 2; ++a)
#pragma unroll
    for (int e = 0; e < 4; ++e) { float v = r[a][e]; s1 += v; s2 = fmaf(v, v, s2); }
  xsum2(s1, s2);
  if (x.lane < 16) st64(x, u32x2{__float_as_uint(s1), __float_as_uint(s2)}, x.lane * 8, WS_XS + L * XS_BUF + (tau * 16 + sblk) * 128);
}

DI void finish_stats(const u32x2 (&p)[4], float& mean, float& rstd) {
  float s1 = 0.f, s2 = 0.f;
#pragma unroll
  for (int u = 0; u < 4; ++u) { s1 += __uint_as_float(p[u].x); s2 += __uint_as_float(p[u].y); }
  xsum2(s1, s2);
  mean = s1 * (1.f / 512.f);
  float var = fmaxf(s2 * (1.f / 512.f) - mean * mean, 0.f);
  rstd = __builtin_amdgcn_rsqf(var + 1e-5f);
}
DI u32x2 ln_pack(f32x4 a, u32x4 csu, float mean, float rstd) {
  const f32x4 cs = __builtin_bit_cast(f32x4, csu);
  float v0 = fmaf(-mean, cs.x, a.x) * rstd, v1 = fmaf(-mean, cs.y, a.y) * rstd;
  float v2 = fmaf(-mean, cs.z, a.z) * rstd, v3 = fmaf(-mean, cs.w, a.w) * rstd;
  return u32x2{pk2(v0, v1), pk2(v2, v3)};
}

DI void feature_map(const u32x2 (&in)[2], const u32x2 (&wp)[2], unsigned (&o)[8]) {
  f32x4 p[4];
  const f32x4 z = {0.f, 0.f, 0.f, 0.f};
#pragma unroll
  for (int h = 0; h < 2; ++h) {
    p[2 * h] = mfma16(wp[h], in[h], z);
    p[2 * h + 1] = mfma16(u32x2{wp[h].x ^ 0x80008000u, wp[h].y ^ 0x80008000u}, in[h], z);
  }
  float v[4][4];
#pragma unroll
  for (int i = 0; i < 4; ++i)
#pragma unroll
    for (int r = 0; r < 4; ++r) v[i][r] = p[i][r];
#pragma unroll
  for (int r = 0; r < 4; ++r) { swap32f(v[0][r], v[2][r]); swap32f(v[1][r], v[3][r]); }
#pragma unroll
  for (int r = 0; r < 4; ++r) { swap16f(v[0][r], v[1][r]); swap16f(v[2][r], v[3][r]); }
  float m4[4];
#pragma unroll
  for (int i = 0; i < 4; ++i) m4[i] = fmaxf(fmaxf(v[i][0], v[i][1]), fmaxf(v[i][2], v[i][3]));
  const float m = fmaxf(fmaxf(m4[0], m4[1]), fmaxf(m4[2], m4[3]));
  const float LOG2E = 1.4426950408889634f;
  const float mm = -m * LOG2E;
  float s4[4];
#pragma unroll
  for (int i = 0; i < 4; ++i) {
#pragma unroll
    for (int r = 0; r < 4; ++r) v[i][r] = __builtin_amdgcn_exp2f(fmaf(v[i][r], LOG2E, mm));
    s4[i] = (v[i][0] + v[i][1]) + (v[i][2] + v[i][3]);
  }
  const float rs = __builtin_amdgcn_rcpf((s4[0] + s4[1]) + (s4[2] + s4[3]));
#pragma unroll
  for (int i = 0; i < 4; ++i) { o[2 * i] = pk2(v[i][0] * rs, v[i][1] * rs); o[2 * i + 1] = pk2(v[i][2] * rs, v[i][3] * rs); }
}

DI void colsum(const Ctx& x, int type) {
  const int w = x.wave;
  if (w >= 1 && w <= 6) {
    const int c = x.lane & 15, g = x.lane >> 4, ct = w - 1;
    int a0 = LDS_W + ct * 1024 + x.lane * 16;
    const u32x4 ones = {0x3f803f80u, 0x3f803f80u, 0x3f803f80u, 0x3f803f80u};
    f32x4 s0 = {0.f, 0.f, 0.f, 0.f}, s1 = s0;
#pragma unroll 1
    for (int s = 0; s < 16; s += 8) {
      u32x4 f[8];
#pragma unroll
      for (int i = 0; i < 8; ++i) f[i] = *(LAS u32x4*)(lds_at(a0) + i * 6144);
#pragma unroll
      for (int i = 0; i < 8; i += 2) { s0 = mfma32(f[i], ones, s0); s1 = mfma32(f[i + 1], ones, s1); }
      a0 += 49152;
    }
    s0 += s1;
    if (c == 0) st128(x, __builtin_bit_cast(u32x4, s0), g * 16, WS_CS + blockIdx.x * 1024 + type * 512 + ct * 64);
  }
}

constexpr int XW = 5;

template <bool IS_B>
DI void qkv_phase(const Ctx& x, int L, const u32x2 (&wp)[2], const u16* Wo, const u16* Wnext, unsigned char* wsp) {
  int lane = x.lane;
  OPAQUE_V(lane);
  const int w = x.wave, c = lane & 15, g = lane >> 4;
  const int q = c >> 2, p = c & 3;
  STAMP(4 + L * 12 + 0);
  wait_flags(x, EDGE_X + L, x.gi, (IS_B && x.gi > 0) ? x.gi - 1 : -1);
  STAMP(4 + L * 12 + 1);
  const int tau = 8 * x.gi + w;
  const int xbase = WS_XF + L * FRAG_BUF + tau * 16384;
  u32x4 xf[16];
#pragma unroll
  for (int s = 0; s < XW; ++s) xf[s] = ld128(x, lane * 16 + (s & 3) * 1024, xbase + (s >> 2) * 4096);
  u32x4 hx[2];
  const bool has_halo = IS_B && x.gi > 0;
  const int ht = 8 * x.gi - 2 + (c >> 3), hc = 8 + (c & 7);
  if (IS_B) {
#pragma unroll
    for (int s = 0; s < 2; ++s) hx[s] = ld128(x, has_halo ? ht * 16384 + (g * 16 + hc) * 16 + s * 1024 : 0x7ffffff0, WS_XF + L * FRAG_BUF + w * 2048);
  }
  int wa0 = LDS_W + lane * 16, wa1 = LDS_W + 49152 + lane * 16;
  OPAQUE_V(wa1);
  f32x4 acc[6];
  f32x4 hacc[4];
#pragma unroll
  for (int ct = 0; ct < 6; ++ct) acc[ct] = f32x4{0.f, 0.f, 0.f, 0.f};
#pragma unroll
  for (int i = 0; i < 4; ++i) hacc[i] = f32x4{0.f, 0.f, 0.f, 0.f};
  u32x4 wf[2][6];
#pragma unroll
  for (int ct = 0; ct < 6; ++ct) wf[0][ct] = *(LAS u32x4*)(lds_at(wa0) + ct * 1024);
  u32x2 sp[4], hsp[4];
  u32x4 cs[6];
  SB();
#pragma unroll
  for (int s = 0; s < 16; ++s) {
    if (s + XW < 16) xf[s + XW] = ld128(x, lane * 16 + ((s + XW) & 3) * 1024, xbase + ((s + XW) >> 2) * 4096);
    if (s + XW == 16) {
#pragma unroll
      for (int u = 0; u < 4; ++u) sp[u] = ld64(x, (4 * g + u) * 128 + c * 8, WS_XS + L * XS_BUF + tau * 2048);
    }
    if (s + XW == 17) {
#pragma unroll
      for (int ct = 0; ct < 6; ++ct) cs[ct] = ld128(x, g * 16 + ct * 64, WS_CS + blockIdx.x * 1024 + (IS_B ? 512 : 0));
    }
    if (IS_B && s + XW == 18) {
#pragma unroll
      for (int u = 0; u < 4; ++u) hsp[u] = ld64(x, has_halo ? ht * 2048 + (4 * g + u) * 128 + hc * 8 : 0x7ffffff0, WS_XS + L * XS_BUF);
    }
    if (s + 1 < 16) {
#pragma unroll
      for (int ct = 0; ct < 6; ++ct) wf[(s + 1) & 1][ct] = *(LAS u32x4*)(lds_at(s + 1 < 8 ? wa0 : wa1) + ((s + 1) & 7) * 6144 + ct * 1024);
    }
#pragma unroll
    for (int ct = 0; ct < 6; ++ct) acc[ct] = mfma32(wf[s & 1][ct], xf[s], acc[ct]);
    if (s + 1 < 16) {
#pragma unroll
      for (int i = 0; i < 6; ++i) {
        __builtin_amdgcn_sched_group_barrier(0x008, 1, 0);
        __builtin_amdgcn_sched_group_barrier(0x100, 1, 0);
      }
    }
    SB();
    if (IS_B) {
      if (w == (s >> 1)) {
#pragma unroll
        for (int i = 0; i < 4; ++i) hacc[i] = mfma32(wf[s & 1][2 + i], hx[s & 1], hacc[i]);
      }
      SB();
    }
  }
  float mean, rstd;
  finish_stats(sp, mean, rstd);
  STAMP(4 + L * 12 + 2);
  const int obase = WS_OF + L * FRAG_BUF + tau * 16384 + x.gj * 1024;
  if (!IS_B) {
    constexpr int PHI = LDS_F, VIM = LDS_F + 16384, KVOP = LDS_F + 24576;
    {
      const int row = w * 16 + c;
      u32x2 kn[2];
#pragma unroll
      for (int h = 0; h < 2; ++h) {
        kn[h] = ln_pack(acc[2 + h], cs[2 + h], mean, rstd);
        *(LAS u32x2*)lds_at(VIM + h * 4096 + row * 32 + g * 8) = ln_pack(acc[4 + h], cs[4 + h], mean, rstd);
      }
      unsigned ph[8];
      feature_map(kn, wp, ph);
      *(LAS u32x4*)lds_at(PHI + g * 4096 + row * 32) = u32x4{ph[0], ph[1], ph[2], ph[3]};
      *(LAS u32x4*)lds_at(PHI + g * 4096 + row * 32 + 16) = u32x4{ph[4], ph[5], ph[6], ph[7]};
    }
    BAR();
    const int e = L >> 1;
    if (w < 4) {
      const u32x4 ones = {0x3f803f80u, 0x3f803f80u, 0x3f803f80u, 0x3f803f80u};
      f32x4 kv = {0.f, 0.f, 0.f, 0.f}, zz = {0.f, 0.f, 0.f, 0.f};
      LAS unsigned char* pa = lds_at(PHI + w * 4096 + (4 * g + q) * 32 + p * 8);
      LAS unsigned char* pb = lds_at(VIM + (w >> 1) * 4096 + (4 * g + q) * 32 + p * 8);
#pragma unroll
      for (int u = 0; u < 4; ++u) {
        u32x4 a = tr_frag(pa + u * 1024, 512), b = tr_frag(pb + u * 1024, 512);
        kv = mfma32(a, b, kv);
        zz = mfma32(a, ones, zz);
      }
      st128(x, __builtin_bit_cast(u32x4, kv), lane * 16, WS_KV + e * KV_BUF + ((x.gj * 16 + x.gi) * 4 + w) * 1024);
      if (c == 0) st128(x, __builtin_bit_cast(u32x4, zz), g * 16, WS_KZ + e * KZ_BUF + ((x.gj * 16 + x.gi) * 4 + w) * 64);
    }
    STAMP(4 + L * 12 + 3);
    DRAIN();
    BAR();
    set_flag(x, EDGE_KV + e, x.gj, x.gi);
    dma_wblk(x, Wnext, w);
    u32x4 pq[2];
    {
      u32x2 qn[2];
#pragma unroll
      for (int h = 0; h < 2; ++h) qn[h] = ln_pack(acc[h], cs[h], mean, rstd);
      unsigned ph[8];
      feature_map(qn, wp, ph);
#pragma unroll
      for (int r = 0; r < 2; ++r) { swap16(ph[r], ph[2 + r]); swap16(ph[4 + r], ph[6 + r]); }
#pragma unroll
      for (int r = 0; r < 2; ++r) { swap32(ph[r], ph[4 + r]); swap32(ph[2 + r], ph[6 + r]); }
      pq[0] = u32x4{ph[0], ph[1], ph[2], ph[3]};
      pq[1] = u32x4{ph[4], ph[5], ph[6], ph[7]};
    }
    STAMP(4 + L * 12 + 4);
    DRAIN();
    relayout_wblk(x, w);
    wait_flags(x, EDGE_KV + e, x.gj, -1);
    STAMP(4 + L * 12 + 5);
    {
      const int wi = w & 3;
      const int voff = w < 4 ? lane * 16 : g * 16;
      const int sbase = w < 4 ? WS_KV + e * KV_BUF + (x.gj * 64 + wi) * 1024 : WS_KZ + e * KZ_BUF + (x.gj * 64 + wi) * 64;
      const int sstr = w < 4 ? 4096 : 256;
      u32x4 a[16];
#pragma unroll
      for (int i = 0; i < 16; ++i) a[i] = ld128(x, voff, sbase + i * sstr);
      SB();
      f32x4 t[4];
#pragma unroll
      for (int i = 0; i < 4; ++i)
        t[i] = (__builtin_bit_cast(f32x4, a[4 * i]) + __builtin_bit_cast(f32x4, a[4 * i + 1])) + (__builtin_bit_cast(f32x4, a[4 * i + 2]) + __builtin_bit_cast(f32x4, a[4 * i + 3]));
      const f32x4 kv = (t[0] + t[1]) + (t[2] + t[3]);
      *(LAS u32x2*)lds_at(KVOP + (wi >> 1) * 2048 + (w >> 2) * 1024 + lane * 16 + (wi & 1) * 8) = u32x2{pk2(kv.x, kv.y), pk2(kv.z, kv.w)};
    }
    BAR();
    {
      const f32x4 z = {0.f, 0.f, 0.f, 0.f};
      unsigned o[4];
#pragma unroll
      for (int h = 0; h < 2; ++h) {
        const u32x4 kvop = *(LAS u32x4*)lds_at(KVOP + h * 2048 + lane * 16);
        const u32x4 zop = *(LAS u32x4*)lds_at(KVOP + h * 2048 + 1024 + lane * 16);
        f32x4 num = mfma32(kvop, pq[h], z), den = mfma32(zop, pq[h], z);
        const float r = __builtin_amdgcn_rcpf(den.x + 1e-6f);
        o[2 * h] = pk2(num.x * r, num.y * r); o[2 * h + 1] = pk2(num.z * r, num.w * r);
      }
      st128(x, u32x4{o[0], o[1], o[2], o[3]}, lane * 16, obase);
    }
    STAMP(4 + L * 12 + 6);
    DRAIN();
  } else {
    constexpr int KIM = LDS_F, VIM = LDS_F + 10240, HP = LDS_F + 20480;
    u32x2 qn[2], kn[2];
#pragma unroll
    for (int i = 0; i < 4; ++i) *(LAS f32x4*)lds_at(HP + (w * 4 + i) * 1024 + lane * 16) = hacc[i];
    const int slot = 2 + w;
#pragma unroll
    for (int h = 0; h < 2; ++h) {
      kn[h] = ln_pack(acc[2 + h], cs[2 + h], mean, rstd);
      *(LAS u32x2*)lds_at(KIM + (slot * 2 + h) * 512 + lane * 8) = kn[h];
      *(LAS u32x2*)lds_at(VIM + (slot * 2 + h) * 512 + c * 32 + g * 8) = ln_pack(acc[4 + h], cs[4 + h], mean, rstd);
    }
    BAR();
    if (w < 4) {
      f32x4 a[8];
#pragma unroll
      for (int i = 0; i < 8; ++i) a[i] = *(LAS f32x4*)lds_at(HP + (i * 4 + w) * 1024 + lane * 16);
      float hm, hr;
      finish_stats(hsp, hm, hr);
      const f32x4 sum = ((a[0] + a[1]) + (a[2] + a[3])) + ((a[4] + a[5]) + (a[6] + a[7]));
      const u32x4 csw = w == 0 ? cs[2] : w == 1 ? cs[3] : w == 2 ? cs[4] : cs[5];
      u32x2 v = ln_pack(sum, csw, hm, hr);
      if (!has_halo) v = u32x2{0u, 0u};
      const int hs = c >> 3, h = w & 1;
      const u32x2 zero = {0u, 0u};
      const int a1 = w < 2 ? KIM + (hs * 2 + h) * 512 + (g * 16 + hc) * 8 : VIM + (hs * 2 + h) * 512 + hc * 32 + g * 8;
      *(LAS u32x2*)lds_at(a1) = v;
      *(LAS u32x2*)lds_at(a1 - (w < 2 ? 64 : 256)) = zero;
    }
#pragma unroll
    for (int h = 0; h < 2; ++h) qn[h] = ln_pack(acc[h], cs[h], mean, rstd);
    const float NEG = -1e30f;
    f32x4 bo, bp;
    const bool noprev = (w < 2) && !has_halo;
#pragma unroll
    for (int r = 0; r < 4; ++r) {
      const int key = 4 * g + r;
      bo[r] = (c - key >= 0 && c - key <= 7) ? 0.f : NEG;
      bp[r] = (key >= c + 9 && !noprev) ? 0.f : NEG;
    }
    BAR();
    {
      const u32x4 ones = {0x3f803f80u, 0x3f803f80u, 0x3f803f80u, 0x3f803f80u};
      const f32x4 z = {0.f, 0.f, 0.f, 0.f};
      const float SC = 0.25f * 1.4426950408889634f;
      unsigned o[4];
#pragma unroll
      for (int h = 0; h < 2; ++h) {
        const u32x2 kp = *(LAS u32x2*)lds_at(KIM + ((slot - 2) * 2 + h) * 512 + lane * 8);
        const u32x2 vo = tr_read(lds_at(VIM + (slot * 2 + h) * 512 + (4 * g + q) * 32 + p * 8));
        const u32x2 vp = tr_read(lds_at(VIM + ((slot - 2) * 2 + h) * 512 + (4 * g + q) * 32 + p * 8));
        f32x4 so = mfma16(kn[h], qn[h], bo), sp_ = mfma16(kp, qn[h], bp);
        float m = fmaxf(fmaxf(fmaxf(so.x, so.y), fmaxf(so.z, so.w)), fmaxf(fmaxf(sp_.x, sp_.y), fmaxf(sp_.z, sp_.w)));
        m = xmax1(m);
        const float mm = -m * SC;
        float e[8];
#pragma unroll
        for (int r = 0; r < 4; ++r) { e[r] = __builtin_amdgcn_exp2f(fmaf(so[r], SC, mm)); e[4 + r] = __builtin_amdgcn_exp2f(fmaf(sp_[r], SC, mm)); }
        const u32x4 P = {pk2(e[0], e[1]), pk2(e[2], e[3]), pk2(e[4], e[5]), pk2(e[6], e[7])};
        f32x4 num = mfma32(u32x4{vo.x, vo.y, vp.x, vp.y}, P, z), den = mfma32(ones, P, z);
        const float r = __builtin_amdgcn_rcpf(den.x);
        o[2 * h] = pk2(num.x * r, num.y * r); o[2 * h + 1] = pk2(num.z * r, num.w * r);
      }
      st128(x, u32x4{o[0], o[1], o[2], o[3]}, lane * 16, obase);
    }
    STAMP(4 + L * 12 + 6);
    if (Wnext) {
      dma_wblk(x, Wnext, w);
      asm volatile("s_waitcnt vmcnt(6)" ::: "memory");
    } else {
      DRAIN();
    }
  }
  BAR();
  set_flag(x, EDGE_O + L, x.gi, x.gj);
  dma_wo(x, Wo);
  if (IS_B && Wnext) {
    if (w == 0) DRAIN(); else asm volatile("s_waitcnt vmcnt(9)" ::: "memory");
    relayout_wblk(x, w);
  }
}

template <bool IS_B>
DI void oproj_phase(const Ctx& x, int L, f32x4 (&res)[2], const u16* Wnext, u16* out, unsigned char* wsp) {
  int lane = x.lane;
  OPAQUE_V(lane);
  const int w = x.wave, c = lane & 15, g = lane >> 4;
  const int q = c >> 2, p = c & 3;
  const int tt = w & 3, kh = w >> 2;
  const int tau = 8 * x.gi + 4 * (x.gj >> 3) + tt;
  STAMP(4 + L * 12 + 7);
  wait_flags(x, EDGE_O + L, x.gi, -1);
  STAMP(4 + L * 12 + 8);
  u32x4 of[8];
  const int obase = WS_OF + L * FRAG_BUF + tau * 16384 + kh * 8192;
#pragma unroll
  for (int s = 0; s < 8; ++s) of[s] = ld128(x, lane * 16 + (s & 3) * 1024, obase + (s >> 2) * 4096);
  f32x4 acc[4];
#pragma unroll
  for (int ct = 0; ct < 4; ++ct) acc[ct] = f32x4{0.f, 0.f, 0.f, 0.f};
  const int sw = ((g & 1) << 1) | (q >> 1);
  const int wb = LDS_F + kh * 32768 + (4 * g + q) * 128 + p * 8;
  u32x4 wf[2][4];
#pragma unroll
  for (int ct = 0; ct < 4; ++ct) wf[0][ct] = tr_frag(lds_at(wb + ((ct ^ sw) << 5)), 2048);
  SB();
#pragma unroll
  for (int s = 0; s < 8; ++s) {
    if (s + 1 < 8) {
#pragma unroll
      for (int ct = 0; ct < 4; ++ct) wf[(s + 1) & 1][ct] = tr_frag(lds_at(wb + ((ct ^ sw) << 5)) + (s + 1) * 4096, 2048);
    }
#pragma unroll
    for (int ct = 0; ct < 4; ++ct) acc[ct] = mfma32(wf[s & 1][ct], of[s], acc[ct]);
    SB();
  }
  STAMP(4 + L * 12 + 9);
#pragma unroll
  for (int a = 0; a < 2; ++a) *(LAS f32x4*)lds_at(LDS_W + ((w ^ 4) + 8) * 6144 + a * 1024 + lane * 16) = kh ? acc[a] : acc[2 + a];
  BAR();
#pragma unroll
  for (int a = 0; a < 2; ++a) {
    const f32x4 other = *(LAS f32x4*)lds_at(LDS_W + (w + 8) * 6144 + a * 1024 + lane * 16);
    const f32x4 mine = kh ? acc[2 + a] : acc[a];
    res[a] += mine + other;
  }
  const int sblk = 2 * (x.gj & 7) + kh;
  if (L < 3) {
    store_x(x, L + 1, res, tau, sblk);
    STAMP(4 + L * 12 + 10);
    dma_wblk(x, Wnext, w + 8);
    asm volatile("s_waitcnt vmcnt(6)" ::: "memory");
    BAR();
    set_flag(x, EDGE_X + L + 1, x.gi, x.gj);
    DRAIN();
    relayout_wblk(x, w + 8);
    if (L == 0) {
      BAR();
      colsum(x, 1);
    }
  } else {
    const int tok = 32 * (tau >> 1) + 2 * c + (tau & 1);
#pragma unroll
    for (int a = 0; a < 2; ++a)
      *(GAS u32x2*)(out + tok * 512 + sblk * 32 + a * 16 + 4 * g) = u32x2{pk2(res[a].x, res[a].y), pk2(res[a].z, res[a].w)};
    STAMP(4 + L * 12 + 10);
  }
}

__global__ void __launch_bounds__(512, 2)
fused_fwd(const u16* __restrict__ x0, const u16* __restrict__ wA, const u16* __restrict__ wphi, const u16* __restrict__ woA,
          const u16* __restrict__ wB, const u16* __restrict__ woB, u16* __restrict__ out, unsigned char* __restrict__ wsp) {
  extern __shared__ __attribute__((aligned(16))) unsigned char smem[];
  Ctx x;
  x.lane = threadIdx.x & 63;
  x.wave = __builtin_amdgcn_readfirstlane(threadIdx.x >> 6);
  const int b = blockIdx.x;
  {
    constexpr int NJ = MAP_NJ, NI = 32 / NJ, XJ = 16 / NJ;
    const int xc = b & 7, r = b >> 3;
    x.gi = (xc / XJ) * NI + r / NJ;
    x.gj = (xc % XJ) * NJ + r % NJ;
  }
  x.rs = __builtin_amdgcn_make_buffer_rsrc((void*)wsp, 0, WS_END, 0x00020000);
  x.flags = (gu64*)(wsp + WS_FLAGS);
  STAMP(0);
  CSTAMP(60);
  const unsigned long long epoch = __hip_atomic_load((gu64*)wsp, RLX_AGENT);
  const int lane = x.lane, w = x.wave, c = lane & 15, g = lane >> 4;
  f32x4 res[2];
  const int tau = 8 * x.gi + 4 * (x.gj >> 3) + (w & 3), sblk = 2 * (x.gj & 7) + (w >> 2);
  {
    const int tok = 32 * (tau >> 1) + 2 * c + (tau & 1);
#pragma unroll
    for (int a = 0; a < 2; ++a) {
      const u32x2 v = *(const GAS u32x2*)(x0 + tok * 512 + sblk * 32 + a * 16 + 4 * g);
      res[a] = f32x4{bflo(v.x), bfhi(v.x), bflo(v.y), bfhi(v.y)};
    }
  }
  u32x2 wp[2];
#pragma unroll
  for (int h = 0; h < 2; ++h) {
    const GAS u16* pw = (const GAS u16*)wphi + ((2 * x.gj + h) * 16 + 4 * g) * 16 + c;
    const unsigned a0 = pw[0], a1 = pw[16], a2 = pw[32], a3 = pw[48];
    wp[h] = u32x2{a0 | (a1 << 16), a2 | (a3 << 16)};
  }
  {
    unsigned wv[3];
#pragma unroll
    for (int k = 0; k < 3; ++k) {
      const int ci = k * 64 + lane, row = ci / 12, pc = ci % 12, lc = pc ^ (((row >> 2) & 1) << 1);
      wv[k] = row * 3072 + (lc >> 2) * 1024 + x.gj * 64 + (lc & 3) * 16;
    }
    x.wv0 = wv[0]; x.wv1 = wv[1]; x.wv2 = wv[2];
    const int r8 = lane >> 3, p8 = lane & 7, lu = (p8 >> 1) ^ ((r8 >> 1) & 3);
    x.wovoff = r8 * 1024 + (x.gj & 7) * 128 + ((lu << 1) | (p8 & 1)) * 16;
  }
  x.tag = __builtin_amdgcn_readfirstlane((unsigned)epoch) | ((unsigned long long)__builtin_amdgcn_readfirstlane((unsigned)(epoch >> 32)) << 32);
  x.tag += 1;
  store_x(x, 0, res, tau, sblk);
  STAMP(1);
  dma_wblk(x, wA, w);
  asm volatile("s_waitcnt vmcnt(6)" ::: "memory");
  BAR();
  set_flag(x, EDGE_X + 0, x.gi, x.gj);
  dma_wblk(x, wA, w + 8);
  asm volatile("s_waitcnt vmcnt(6)" ::: "memory");
  relayout_wblk(x, w);
  DRAIN();
  relayout_wblk(x, w + 8);
  BAR();
  STAMP(2);
  colsum(x, 0);
#pragma unroll 1
  for (int it = 0; it < 2; ++it) {
    qkv_phase<false>(x, 2 * it, wp, woA, wB, wsp);
    oproj_phase<false>(x, 2 * it, res, wB, out, wsp);
    if (it == 0 && b == 0 && threadIdx.x == 0) __hip_atomic_store((gu64*)wsp, x.tag, RLX_AGENT);
    qkv_phase<true>(x, 2 * it + 1, wp, woB, it == 0 ? wA : nullptr, wsp);
    oproj_phase<true>(x, 2 * it + 1, res, wA, out, wsp);
  }
  STAMP(3);
  CSTAMP(61);
}

extern "C" void kernel_launch(void* const* d_in, const int* in_sizes, int n_in, void* d_out, int out_size, void* d_ws, size_t ws_size,
                              hipStream_t stream) {
  (void)in_sizes; (void)n_in; (void)out_size; (void)ws_size;
  (void)hipFuncSetAttribute((const void*)fused_fwd, hipFuncAttributeMaxDynamicSharedMemorySize, LDS_BYTES);
  fused_fwd<<<256, 512, LDS_BYTES, stream>>>((const u16*)d_in[0], (const u16*)d_in[1], (const u16*)d_in[2], (const u16*)d_in[3],
                                             (const u16*)d_in[5], (const u16*)d_in[6], (u16*)d_out, (unsigned char*)d_ws);
}
```

```cpp
#include <hip/hip_runtime.h>
#include <stdint.h>

typedef unsigned short u16;
typedef __bf16 bf16x8 __attribute__((ext_vector_type(8)));
typedef __bf16 bf16x2 __attribute__((ext_vector_type(2)));
typedef short s16x4 __attribute__((ext_vector_type(4)));
typedef float f32x4 __attribute__((ext_vector_type(4)));
typedef float f32x2 __attribute__((ext_vector_type(2)));
typedef unsigned u32x2 __attribute__((ext_vector_type(2)));
typedef unsigned u32x4 __attribute__((ext_vector_type(4)));
#define LAS __attribute__((address_space(3)))
#define GAS __attribute__((address_space(1)))
typedef GAS unsigned long long gu64;
#define RLX_AGENT __ATOMIC_RELAXED, __HIP_MEMORY_SCOPE_AGENT
#define DI __device__ __forceinline__

constexpr int WS_FLAGS = 4096;
constexpr int EDGE_X = 0, EDGE_O = 4, EDGE_KV = 8;
constexpr int WS_XS = 65536;
constexpr int XS_BUF = 128 * 1024;
constexpr int WS_KZ = WS_XS + 2 * XS_BUF;
constexpr int KZ_BUF = 16 * 4 * 1024;
constexpr int FRAG_BUF = 2 << 20;
constexpr int WS_XF = 1 << 20;
constexpr int WS_OF = WS_XF + 2 * FRAG_BUF;
constexpr int WS_KV = WS_OF + FRAG_BUF;
constexpr int KV_BUF = 16 * 16 * 4 * 1024;
constexpr int WS_END = WS_KV + 2 * KV_BUF;
constexpr int WS_STAMP = 16 << 20;

constexpr int LDS_W = 0;
constexpr int LDS_F = 98304;
constexpr int LDS_CS = LDS_F;
constexpr int LDS_S = LDS_F + 512;
constexpr int LDS_BYTES = 163840;
constexpr int AUX_SC1 = 16;
#define MAP_NJ 8
constexpr unsigned SPIN_CAP = 1u << 16;

DI unsigned pk2(float a, float b) { f32x2 v = {a, b}; return __builtin_bit_cast(unsigned, __builtin_convertvector(v, bf16x2)); }
DI float bflo(unsigned u) { return __uint_as_float(u << 16); }
DI float bfhi(unsigned u) { return __uint_as_float(u & 0xffff0000u); }
DI f32x4 mfma32(u32x4 a, u32x4 b, f32x4 c) {
  return __builtin_amdgcn_mfma_f32_16x16x32_bf16(__builtin_bit_cast(bf16x8, a), __builtin_bit_cast(bf16x8, b), c, 0, 0, 0);
}
DI f32x4 mfma16(u32x2 a, u32x2 b, f32x4 c) {
  return __builtin_amdgcn_mfma_f32_16x16x16bf16_1k(__builtin_bit_cast(s16x4, a), __builtin_bit_cast(s16x4, b), c, 0, 0, 0);
}
DI LAS unsigned char* lds_at(int off) { return (LAS unsigned char*)(uintptr_t)(unsigned)off; }
DI u32x2 tr_read(LAS unsigned char* p) { return __builtin_bit_cast(u32x2, __builtin_amdgcn_ds_read_tr16_b64_v4i16((LAS s16x4*)p)); }
DI u32x4 tr_frag(LAS unsigned char* p, int second) {
  u32x2 a = tr_read(p), b = tr_read(p + second);
  return u32x4{a.x, a.y, b.x, b.y};
}
DI void swap32(unsigned& a, unsigned& b) { auto r = __builtin_amdgcn_permlane32_swap(a, b, false, false); a = r[0]; b = r[1]; }
DI void swap16(unsigned& a, unsigned& b) { auto r = __builtin_amdgcn_permlane16_swap(a, b, false, false); a = r[0]; b = r[1]; }
DI void swap32f(float& a, float& b) { unsigned x = __float_as_uint(a), y = __float_as_uint(b); swap32(x, y); a = __uint_as_float(x); b = __uint_as_float(y); }
DI void swap16f(float& a, float& b) { unsigned x = __float_as_uint(a), y = __float_as_uint(b); swap16(x, y); a = __uint_as_float(x); b = __uint_as_float(y); }
DI float max3(float a, float b, float c) { return __builtin_fmaxf(__builtin_fmaxf(a, b), c); }
DI void xsum2(float& s1, float& s2) {
  swap32f(s1, s2);
  float t = s1 + s2, u = t;
  swap16f(t, u);
  float a = t + u, b = a;
  swap32f(a, b);
  s1 = a; s2 = b;
}
#define BAR() asm volatile("s_waitcnt lgkmcnt(0)\n\ts_barrier" ::: "memory")
#define DRAIN() asm volatile("s_waitcnt vmcnt(0)" ::: "memory")
#define SB() __builtin_amdgcn_sched_barrier(0)
#define OPAQUE_V(x) asm volatile("" : "+v"(x))
#define OPAQUE_S(x) asm volatile("" : "+s"(x))

DI void dma16(const void* sbase, unsigned voff, unsigned lds_addr) {
  unsigned keep;
  asm volatile("s_mov_b32 %0, m0\n\ts_mov_b32 m0, %3\n\ts_nop 2\n\tglobal_load_lds_dwordx4 %1, %2\n\ts_mov_b32 m0, %0"
               : "=&s"(keep) : "v"(voff), "s"(sbase), "s"(lds_addr) : "memory");
}

struct Ctx {
  __amdgpu_buffer_rsrc_t rs;
  gu64* flags;
  unsigned long long tag;
  int lane, wave, gi, gj;
  unsigned wv0, wv1, wv2;
};

DI u32x4 ld128(const Ctx& x, int voff, int soff) { return __builtin_amdgcn_raw_buffer_load_b128(x.rs, voff, soff, AUX_SC1); }
DI void st128(const Ctx& x, u32x4 v, int voff, int soff) { __builtin_amdgcn_raw_buffer_store_b128(v, x.rs, voff + soff, 0, AUX_SC1); }
DI void st64(const Ctx& x, u32x2 v, int voff, int soff) { __builtin_amdgcn_raw_buffer_store_b64(v, x.rs, voff, soff, AUX_SC1); }
DI void st32(const Ctx& x, unsigned v, int voff, int soff) { __builtin_amdgcn_raw_buffer_store_b32(v, x.rs, voff, soff, AUX_SC1); }

#define STAMP(n) do {} while (0)
#define CSTAMP(n) do {} while (0)

DI void dma_wblk(const Ctx& x, const u16* W, int s) {
  const char* src = (const char*)W + (size_t)s * 98304;
  const unsigned dst = LDS_W + s * 6144;
  dma16(src, x.wv0, dst); dma16(src, x.wv1, dst + 1024); dma16(src, x.wv2, dst + 2048);
  src += 49152;
  dma16(src, x.wv0, dst + 3072); dma16(src, x.wv1, dst + 4096); dma16(src, x.wv2, dst + 5120);
}
DI void relayout_wblk(const Ctx& x, int s) {
  int lane = x.lane;
  OPAQUE_V(lane);
  const int c = lane & 15, g = lane >> 4;
  const int base = LDS_W + s * 6144;
  const int a = base + (4 * g + (c >> 2)) * 192 + (c & 3) * 8;
  u32x4 f[6];
#pragma unroll
  for (int ct = 0; ct < 6; ++ct) f[ct] = tr_frag(lds_at(a + ((ct & 1) ? -(g & 1) * 32 : (g & 1) * 32)) + ct * 32, 3072);
  asm volatile("s_waitcnt lgkmcnt(0)" : "+v"(f[0]), "+v"(f[1]), "+v"(f[2]), "+v"(f[3]), "+v"(f[4]), "+v"(f[5]) :: "memory");
#pragma unroll
  for (int ct = 0; ct < 6; ++ct) *(LAS u32x4*)(lds_at(base + lane * 16) + ct * 1024) = f[ct];
}
DI void dma_wo(const Ctx& x, const u16* Wo, unsigned dst0, unsigned dst1) {
  int lane = x.lane;
  OPAQUE_V(lane);
  const int r8 = lane >> 3, p8 = lane & 7, lu = (p8 >> 1) ^ ((r8 >> 1) & 3);
  const unsigned voff = r8 * 1024 + (x.gj & 7) * 128 + ((lu << 1) | (p8 & 1)) * 16;
  const char* src = (const char*)Wo + (size_t)x.wave * 65536;
#pragma unroll
  for (int n = 0; n < 8; ++n) dma16(src + n * 8192, voff, (n < 4 ? dst0 : dst1) + (n & 3) * 1024);
}
DI void fetch_wo(const Ctx& x, unsigned dst0, unsigned dst1, u32x4 (&po)[8]) {
  int lane = x.lane;
  OPAQUE_V(lane);
  const int c = lane & 15, g = lane >> 4, q = c >> 2;
  const int sw = ((g & 1) << 1) | (q >> 1);
  const int a = (4 * g + q) * 128 + (c & 3) * 8;
#pragma unroll
  for (int n = 0; n < 8; ++n) po[n] = tr_frag(lds_at((n < 4 ? dst0 : dst1) + a + (((n & 3) ^ sw) << 5)), 2048);
}

DI void set_flag(const Ctx& x, int edge, int line, int slot) {
  if (threadIdx.x == 0) __hip_atomic_store(x.flags + (edge * 16 + line) * 16 + slot, x.tag, RLX_AGENT);
}
DI void wait_flags(const Ctx& x, int edge, int line, int line2) {
  if (x.wave == 0) {
    int idx = (edge * 16 + line) * 16 + (x.lane & 15);
    if (line2 >= 0 && x.lane >= 16 && x.lane < 24) idx = (edge * 16 + line2) * 16 + (x.lane - 8);
    gu64* p = x.flags + idx;
    for (unsigned spins = 0; spins < SPIN_CAP; ++spins) {
      unsigned long long v = __hip_atomic_load(p, RLX_AGENT);
      if (__all(v == x.tag)) break;
    }
    __builtin_amdgcn_fence(__ATOMIC_ACQUIRE, "wavefront");
  }
  BAR();
}

DI void store_x(const Ctx& x, int lane, int buf, const f32x4 (&r)[4], int tau, int nb) {
#pragma unroll
  for (int a = 0; a < 2; ++a) {
    u32x4 f = {pk2(r[2 * a].x, r[2 * a].y), pk2(r[2 * a].z, r[2 * a].w), pk2(r[2 * a + 1].x, r[2 * a + 1].y), pk2(r[2 * a + 1].z, r[2 * a + 1].w)};
    st128(x, f, lane * 16 + a * 1024, WS_XF + buf * FRAG_BUF + (tau * 16 + nb * 2) * 1024);
  }
  const f32x4 s = (r[0] + r[1]) + (r[2] + r[3]);
  const f32x4 q = (r[0] * r[0] + r[1] * r[1]) + (r[2] * r[2] + r[3] * r[3]);
  float s1 = (s.x + s.y) + (s.z + s.w), s2 = (q.x + q.y) + (q.z + q.w);
  xsum2(s1, s2);
  if (lane < 16) st64(x, u32x2{__float_as_uint(s1), __float_as_uint(s2)}, lane * 16, WS_XS + buf * XS_BUF + tau * 1024 + (nb >> 1) * 256 + (nb & 1) * 8);
}

DI void finish_stats(u32x4 p, float& mean, float& rstd) {
  float s1 = __uint_as_float(p.x) + __uint_as_float(p.z), s2 = __uint_as_float(p.y) + __uint_as_float(p.w);
  xsum2(s1, s2);
  mean = s1 * (1.f / 512.f);
  float var = fmaxf(s2 * (1.f / 512.f) - mean * mean, 0.f);
  rstd = __builtin_amdgcn_rsqf(var + 1e-5f);
}
DI u32x2 ln_pack(f32x4 a, f32x4 cs, float mean, float rstd) {
  float v0 = fmaf(-mean, cs.x, a.x) * rstd, v1 = fmaf(-mean, cs.y, a.y) * rstd;
  float v2 = fmaf(-mean, cs.z, a.z) * rstd, v3 = fmaf(-mean, cs.w, a.w) * rstd;
  return u32x2{pk2(v0, v1), pk2(v2, v3)};
}

DI void feature_map(const u32x2 (&in)[2], const u32x2 (&wp)[2], unsigned (&o)[8]) {
  f32x4 p[4];
  const f32x4 z = {0.f, 0.f, 0.f, 0.f};
#pragma unroll
  for (int h = 0; h < 2; ++h) {
    p[2 * h] = mfma16(wp[h], in[h], z);
    p[2 * h + 1] = mfma16(u32x2{wp[h].x ^ 0x80008000u, wp[h].y ^ 0x80008000u}, in[h], z);
  }
  float v[16];
#pragma unroll
  for (int i = 0; i < 4; ++i)
#pragma unroll
    for (int r = 0; r < 4; ++r) v[4 * i + r] = p[i][r];
#pragma unroll
  for (int r = 0; r < 4; ++r) { swap32f(v[r], v[8 + r]); swap32f(v[4 + r], v[12 + r]); }
#pragma unroll
  for (int r = 0; r < 4; ++r) { swap16f(v[r], v[4 + r]); swap16f(v[8 + r], v[12 + r]); }
  const float m = max3(max3(max3(v[0], v[1], v[2]), max3(v[3], v[4], v[5]), max3(v[6], v[7], v[8])),
                       max3(max3(v[9], v[10], v[11]), max3(v[12], v[13], v[14]), v[15]), v[15]);
  const float LOG2E = 1.4426950408889634f;
  const float mm = -m * LOG2E;
#pragma unroll
  for (int i = 0; i < 16; ++i) v[i] = __builtin_amdgcn_exp2f(fmaf(v[i], LOG2E, mm));
  f32x2 s2 = {0.f, 0.f};
#pragma unroll
  for (int i = 0; i < 16; i += 4) s2 += (f32x2{v[i], v[i + 1]} + f32x2{v[i + 2], v[i + 3]});
  const float rs = __builtin_amdgcn_rcpf(s2.x + s2.y);
#pragma unroll
  for (int i = 0; i < 8; ++i) { const f32x2 t = f32x2{v[2 * i], v[2 * i + 1]} * rs; o[i] = pk2(t.x, t.y); }
}

DI f32x4 colsum(const Ctx& x) {
  f32x4 s0 = {0.f, 0.f, 0.f, 0.f}, s1 = s0;
  const int w = x.wave;
  if (w >= 1 && w <= 6) {
    int a0 = LDS_W + (w - 1) * 1024 + x.lane * 16;
    const u32x4 ones = {0x3f803f80u, 0x3f803f80u, 0x3f803f80u, 0x3f803f80u};
#pragma unroll 1
    for (int s = 0; s < 16; s += 8) {
      u32x4 f[8];
#pragma unroll
      for (int i = 0; i < 8; ++i) f[i] = *(LAS u32x4*)(lds_at(a0) + i * 6144);
#pragma unroll
      for (int i = 0; i < 8; i += 2) { s0 = mfma32(f[i], ones, s0); s1 = mfma32(f[i + 1], ones, s1); }
      a0 += 49152;
    }
  }
  return s0 + s1;
}

#define XWIN 6
constexpr int XW = XWIN;

#define LDX(s) ld128(x, lane * 16 + ((s) & 3) * 1024, xbase + ((s) >> 2) * 4096)
template <bool IS_B>
DI void qkv_phase(const Ctx& x, int L, const u32x2 (&wp)[2], f32x4 cskeep, const u32x4 (&po)[8], u32x4 (&po_other)[8], const u16* WoOther,
                  const u16* Wnext, unsigned char* wsp) {
  int lane = x.lane, w = x.wave, gi = x.gi, gj = x.gj;
  OPAQUE_V(lane); OPAQUE_S(w); OPAQUE_S(gi); OPAQUE_S(gj);
  const int c = lane & 15, g = lane >> 4;
  const int q = c >> 2, p = c & 3;
  if (w >= 1 && w <= 6 && c == 0) *(LAS f32x4*)lds_at(LDS_CS + (w - 1) * 64 + g * 16) = cskeep;
  STAMP(4 + L * 12 + 0);
  wait_flags(x, EDGE_X + L, gi, (IS_B && gi > 0) ? gi - 1 : -1);
  STAMP(4 + L * 12 + 1);
  constexpr int XB = IS_B ? 1 : 0;
  const int tau = 8 * gi + w;
  const int xbase = WS_XF + XB * FRAG_BUF + tau * 16384;
  u32x4 hx[2], hsp;
  const bool has_halo = IS_B && gi > 0;
  const int ht = 8 * gi - 2 + (c >> 3), hc = 8 + (c & 7);
  if (IS_B) {
#pragma unroll
    for (int s = 0; s < 2; ++s) hx[s] = ld128(x, has_halo ? ht * 16384 + (g * 16 + hc) * 16 + s * 1024 : 0x7ffffff0, WS_XF + XB * FRAG_BUF + w * 2048);
  }
  u32x4 xf[16];
#pragma unroll
  for (int s = 0; s < XW; ++s) xf[s] = LDX(s);
  const u32x4 sp = ld128(x, lane * 16, WS_XS + XB * XS_BUF + tau * 1024);
  constexpr int KIM = LDS_S, VIMB = KIM + 10240, HP = VIMB + 10240;
  if (IS_B) {
    if (w < 4) hsp = ld128(x, has_halo ? ht * 1024 + (g * 16 + hc) * 16 : 0x7ffffff0, WS_XS + XB * XS_BUF);
    u32x4 f[8];
#pragma unroll
    for (int i = 0; i < 8; ++i) f[i] = *(LAS u32x4*)(lds_at(LDS_W + w * 12288 + 2048 + lane * 16) + (i >> 2) * 6144 + (i & 3) * 1024);
#pragma unroll
    for (int i = 0; i < 4; ++i) {
      f32x4 h = mfma32(f[i], hx[0], f32x4{0.f, 0.f, 0.f, 0.f});
      h = mfma32(f[4 + i], hx[1], h);
      *(LAS f32x4*)(lds_at(HP + w * 4096 + lane * 16) + i * 1024) = h;
    }
  }
  int wa0 = LDS_W + lane * 16, wa1 = LDS_W + 49152 + lane * 16;
  OPAQUE_V(wa1);
  constexpr int C0 = IS_B ? 0 : 2;
  constexpr int NC = 6 - C0;
  f32x4 acc[6];
#pragma unroll
  for (int ct = 0; ct < 6; ++ct) acc[ct] = f32x4{0.f, 0.f, 0.f, 0.f};
  u32x4 wf[2][6];
#pragma unroll
  for (int ct = C0; ct < 6; ++ct) wf[0][ct] = *(LAS u32x4*)(lds_at(wa0) + ct * 1024);
  float mean, rstd;
  SB();
#pragma unroll
  for (int s = 0; s < 16; ++s) {
    if (s + XW < 16) xf[s + XW] = LDX(s + XW);
    if (s + 1 < 16) {
#pragma unroll
      for (int ct = C0; ct < 6; ++ct) wf[(s + 1) & 1][ct] = *(LAS u32x4*)(lds_at(s + 1 < 8 ? wa0 : wa1) + ((s + 1) & 7) * 6144 + ct * 1024);
    }
#pragma unroll
    for (int ct = C0; ct < 6; ++ct) acc[ct] = mfma32(wf[s & 1][ct], xf[s], acc[ct]);
    if (s == 10) finish_stats(sp, mean, rstd);
    if (s + 1 < 16) {
#pragma unroll
      for (int i = 0; i < NC; ++i) {
        __builtin_amdgcn_sched_group_barrier(0x008, 1, 0);
        __builtin_amdgcn_sched_group_barrier(0x100, 1, 0);
        if (s == 10) __builtin_amdgcn_sched_group_barrier(0x002, 5, 0);
      }
    }
    SB();
  }
  f32x4 cs[6];
#pragma unroll
  for (int ct = C0; ct < 6; ++ct) cs[ct] = *(LAS f32x4*)(lds_at(LDS_CS + g * 16) + ct * 64);
  STAMP(4 + L * 12 + 2);
  const int obase = WS_OF + tau * 16384 + gj * 1024;
  if (!IS_B) {
    constexpr int PHI = LDS_S, VIM = PHI + 16384, KVOP = VIM + 8192, ZOP = KVOP + 2048;
    {
      const int row = w * 16 + c;
      u32x2 kn[2];
#pragma unroll
      for (int h = 0; h < 2; ++h) kn[h] = ln_pack(acc[2 + h], cs[2 + h], mean, rstd);
      unsigned ph[8];
      feature_map(kn, wp, ph);
#pragma unroll
      for (int h = 0; h < 2; ++h) *(LAS u32x2*)lds_at(VIM + h * 4096 + row * 32 + g * 8) = ln_pack(acc[4 + h], cs[4 + h], mean, rstd);
      *(LAS u32x4*)lds_at(PHI + g * 4096 + row * 32) = u32x4{ph[0], ph[1], ph[2], ph[3]};
      *(LAS u32x4*)lds_at(PHI + g * 4096 + row * 32 + 16) = u32x4{ph[4], ph[5], ph[6], ph[7]};
    }
    BAR();
    const int e = L >> 1;
    if (w < 4) {
      const u32x4 ones = {0x3f803f80u, 0x3f803f80u, 0x3f803f80u, 0x3f803f80u};
      f32x4 kv0 = {0.f, 0.f, 0.f, 0.f}, kv1 = kv0, z0 = kv0, z1 = kv0;
      LAS unsigned char* pa = lds_at(PHI + w * 4096 + (4 * g + q) * 32 + p * 8);
      LAS unsigned char* pb = lds_at(VIM + (w >> 1) * 4096 + (4 * g + q) * 32 + p * 8);
      u32x4 a[4], b[4];
#pragma unroll
      for (int u = 0; u < 4; ++u) { a[u] = tr_frag(pa + u * 1024, 512); b[u] = tr_frag(pb + u * 1024, 512); }
      kv0 = mfma32(a[0], b[0], kv0); kv1 = mfma32(a[1], b[1], kv1); kv0 = mfma32(a[2], b[2], kv0); kv1 = mfma32(a[3], b[3], kv1);
      z0 = mfma32(a[0], ones, z0); z1 = mfma32(a[1], ones, z1); z0 = mfma32(a[2], ones, z0); z1 = mfma32(a[3], ones, z1);
      kv0 += kv1; z0 += z1;
      st128(x, __builtin_bit_cast(u32x4, kv0), lane * 16, WS_KV + e * KV_BUF + ((gj * 16 + gi) * 4 + w) * 1024);
      const float zv = c == 0 ? z0.x : c == 1 ? z0.y : c == 2 ? z0.z : z0.w;
      if (c < 4) st32(x, __float_as_uint(zv), (4 * g + c) * 64, WS_KZ + e * KZ_BUF + (gj * 4 + w) * 1024 + gi * 4);
      DRAIN();
    }
    STAMP(4 + L * 12 + 3);
    BAR();
    set_flag(x, EDGE_KV + e, gj, gi);
    if (L == 0) dma_wo(x, WoOther, LDS_W + 2 * w * 6144 + 2048, LDS_W + (2 * w + 1) * 6144 + 2048);
    u32x4 pq[2];
    {
#pragma unroll
      for (int s = 0; s < 16; s += 4) {
        u32x4 f[8];
#pragma unroll
        for (int i = 0; i < 8; ++i) f[i] = *(LAS u32x4*)(lds_at(s < 8 ? wa0 : wa1) + ((s + (i >> 1)) & 7) * 6144 + (i & 1) * 1024);
#pragma unroll
        for (int i = 0; i < 8; ++i) acc[i & 1] = mfma32(f[i], xf[s + (i >> 1)], acc[i & 1]);
      }
      u32x2 qn[2];
#pragma unroll
      for (int h = 0; h < 2; ++h) qn[h] = ln_pack(acc[h], *(LAS f32x4*)(lds_at(LDS_CS + g * 16) + h * 64), mean, rstd);
      unsigned ph[8];
      feature_map(qn, wp, ph);
#pragma unroll
      for (int r = 0; r < 2; ++r) { swap16(ph[r], ph[2 + r]); swap16(ph[4 + r], ph[6 + r]); }
#pragma unroll
      for (int r = 0; r < 2; ++r) { swap32(ph[r], ph[4 + r]); swap32(ph[2 + r], ph[6 + r]); }
      pq[0] = u32x4{ph[0], ph[1], ph[2], ph[3]};
      pq[1] = u32x4{ph[4], ph[5], ph[6], ph[7]};
    }
    if (L == 0) {
      DRAIN();
      fetch_wo(x, LDS_W + 2 * w * 6144 + 2048, LDS_W + (2 * w + 1) * 6144 + 2048, po_other);
    }
    STAMP(4 + L * 12 + 4);
    wait_flags(x, EDGE_KV + e, gj, -1);
    STAMP(4 + L * 12 + 5);
    if (w < 4) {
      const int sbase = WS_KV + e * KV_BUF + (gj * 64 + w) * 1024;
      u32x4 a[16];
#pragma unroll
      for (int i = 0; i < 16; ++i) a[i] = ld128(x, lane * 16, sbase + i * 4096);
      SB();
      f32x4 t[4];
#pragma unroll
      for (int i = 0; i < 4; ++i)
        t[i] = (__builtin_bit_cast(f32x4, a[4 * i]) + __builtin_bit_cast(f32x4, a[4 * i + 1])) + (__builtin_bit_cast(f32x4, a[4 * i + 2]) + __builtin_bit_cast(f32x4, a[4 * i + 3]));
      const f32x4 kv = (t[0] + t[1]) + (t[2] + t[3]);
      *(LAS u32x2*)lds_at(KVOP + (w >> 1) * 1024 + lane * 16 + (w & 1) * 8) = u32x2{pk2(kv.x, kv.y), pk2(kv.z, kv.w)};
    } else {
      const int wi = w - 4;
      const f32x4 a = __builtin_bit_cast(f32x4, ld128(x, lane * 16, WS_KZ + e * KZ_BUF + (gj * 4 + wi) * 1024));
      float s = (a.x + a.y) + (a.z + a.w);
      s += __builtin_bit_cast(float, __builtin_amdgcn_update_dpp(0, __builtin_bit_cast(int, s), 0xB1, 0xf, 0xf, true));
      s += __builtin_bit_cast(float, __builtin_amdgcn_update_dpp(0, __builtin_bit_cast(int, s), 0x4E, 0xf, 0xf, true));
      const int f = lane >> 2;
      if ((lane & 3) == 0) *(LAS u16*)lds_at(ZOP + (wi >> 1) * 64 + (f >> 2) * 16 + (wi & 1) * 8 + (f & 3) * 2) = (u16)pk2(s, s);
    }
    BAR();
    {
      const f32x4 z = {0.f, 0.f, 0.f, 0.f};
      unsigned o[4];
#pragma unroll
      for (int h = 0; h < 2; ++h) {
        const u32x4 kvop = *(LAS u32x4*)lds_at(KVOP + h * 1024 + lane * 16);
        const u32x4 zop = *(LAS u32x4*)lds_at(ZOP + h * 64 + g * 16);
        f32x4 num = mfma32(kvop, pq[h], z), den = mfma32(zop, pq[h], z);
        const float r = __builtin_amdgcn_rcpf(den.x + 1e-6f);
        o[2 * h] = pk2(num.x * r, num.y * r); o[2 * h + 1] = pk2(num.z * r, num.w * r);
      }
      st128(x, u32x4{o[0], o[1], o[2], o[3]}, lane * 16, obase);
    }
  } else {
    constexpr int VIM = VIMB;
    u32x2 qn[2], kn[2];
    const int slot = 2 + w;
#pragma unroll
    for (int h = 0; h < 2; ++h) {
      kn[h] = ln_pack(acc[2 + h], cs[2 + h], mean, rstd);
      *(LAS u32x2*)lds_at(KIM + (slot * 2 + h) * 512 + lane * 8) = kn[h];
      *(LAS u32x2*)lds_at(VIM + (slot * 2 + h) * 512 + c * 32 + g * 8) = ln_pack(acc[4 + h], cs[4 + h], mean, rstd);
    }
    float hm = 0.f, hr = 0.f;
    if (w < 4) finish_stats(hsp, hm, hr);
    BAR();
    if (w < 4) {
      f32x4 a[8];
#pragma unroll
      for (int i = 0; i < 8; ++i) a[i] = *(LAS f32x4*)lds_at(HP + (i * 4 + w) * 1024 + lane * 16);
      const f32x4 csw = *(LAS f32x4*)lds_at(LDS_CS + (2 + w) * 64 + g * 16);
      const f32x4 sum = ((a[0] + a[1]) + (a[2] + a[3])) + ((a[4] + a[5]) + (a[6] + a[7]));
      u32x2 v = ln_pack(sum, csw, hm, hr);
      if (!has_halo) v = u32x2{0u, 0u};
      const int hs = c >> 3, h = w & 1;
      const u32x2 zero = {0u, 0u};
      const int a1 = w < 2 ? KIM + (hs * 2 + h) * 512 + (g * 16 + hc) * 8 : VIM + (hs * 2 + h) * 512 + hc * 32 + g * 8;
      *(LAS u32x2*)lds_at(a1) = v;
      *(LAS u32x2*)lds_at(a1 - (w < 2 ? 64 : 256)) = zero;
    }
#pragma unroll
    for (int h = 0; h < 2; ++h) qn[h] = ln_pack(acc[h], cs[h], mean, rstd);
    const float NEG = -1e30f;
    f32x4 bo, bp;
    const bool noprev = (w < 2) && !has_halo;
#pragma unroll
    for (int r = 0; r < 4; ++r) {
      const int key = 4 * g + r;
      bo[r] = (c - key >= 0 && c - key <= 7) ? 0.f : NEG;
      bp[r] = (key >= c + 9 && !noprev) ? 0.f : NEG;
    }
    f32x4 so[2];
    u32x2 vo[2];
#pragma unroll
    for (int h = 0; h < 2; ++h) {
      so[h] = mfma16(kn[h], qn[h], bo);
      vo[h] = tr_read(lds_at(VIM + (slot * 2 + h) * 512 + (4 * g + q) * 32 + p * 8));
    }
    BAR();
    {
      const u32x4 ones = {0x3f803f80u, 0x3f803f80u, 0x3f803f80u, 0x3f803f80u};
      const f32x4 z = {0.f, 0.f, 0.f, 0.f};
      const float SC = 0.25f * 1.4426950408889634f;
      f32x4 sp_[2];
      u32x2 vp[2];
      float m[2];
#pragma unroll
      for (int h = 0; h < 2; ++h) {
        const u32x2 kp = *(LAS u32x2*)lds_at(KIM + ((slot - 2) * 2 + h) * 512 + lane * 8);
        vp[h] = tr_read(lds_at(VIM + ((slot - 2) * 2 + h) * 512 + (4 * g + q) * 32 + p * 8));
        sp_[h] = mfma16(kp, qn[h], bp);
        m[h] = max3(max3(so[h].x, so[h].y, so[h].z), max3(so[h].w, sp_[h].x, sp_[h].y), max3(sp_[h].z, sp_[h].w, sp_[h].w));
      }
      swap32f(m[0], m[1]);
      float t = max3(m[0], m[1], m[1]), u = t;
      swap16f(t, u);
      float a = max3(t, u, u), b = a;
      swap32f(a, b);
      m[0] = a; m[1] = b;
      unsigned o[4];
#pragma unroll
      for (int h = 0; h < 2; ++h) {
        const float mm = -m[h] * SC;
        float e[8];
#pragma unroll
        for (int r = 0; r < 4; ++r) { e[r] = __builtin_amdgcn_exp2f(fmaf(so[h][r], SC, mm)); e[4 + r] = __builtin_amdgcn_exp2f(fmaf(sp_[h][r], SC, mm)); }
        const u32x4 P = {pk2(e[0], e[1]), pk2(e[2], e[3]), pk2(e[4], e[5]), pk2(e[6], e[7])};
        f32x4 num = mfma32(u32x4{vo[h].x, vo[h].y, vp[h].x, vp[h].y}, P, z), den = mfma32(ones, P, z);
        const float r = __builtin_amdgcn_rcpf(den.x);
        o[2 * h] = pk2(num.x * r, num.y * r); o[2 * h + 1] = pk2(num.z * r, num.w * r);
      }
      st128(x, u32x4{o[0], o[1], o[2], o[3]}, lane * 16, obase);
    }
  }
  STAMP(4 + L * 12 + 6);
  DRAIN();
  BAR();
  set_flag(x, EDGE_O + L, gi, gj);
#pragma unroll
  for (int n = 0; n < 8; ++n) *(LAS u32x4*)(lds_at(LDS_F + w * 8192 + lane * 16) + n * 1024) = po[n];
  if (Wnext && w > 0) dma_wblk(x, Wnext, w - 1);
}

DI void oproj_phase(const Ctx& x, int L, f32x4 (&res)[4], f32x4& csB, const u16* Wnext, u16* out, unsigned char* wsp) {
  int lane = x.lane, w = x.wave, gi = x.gi, gj = x.gj;
  OPAQUE_V(lane); OPAQUE_S(w); OPAQUE_S(gi); OPAQUE_S(gj);
  const int c = lane & 15, g = lane >> 4;
  const int tt = w & 3, kh = w >> 2;
  const int tau = 8 * gi + 4 * (gj >> 3) + tt;
  STAMP(4 + L * 12 + 7);
  wait_flags(x, EDGE_O + L, gi, -1);
  STAMP(4 + L * 12 + 8);
  u32x4 of[8];
  const int obase = WS_OF + tau * 16384 + kh * 8192;
#pragma unroll
  for (int s = 0; s < 8; ++s) of[s] = ld128(x, lane * 16 + (s & 3) * 1024, obase + (s >> 2) * 4096);
  f32x4 acc[4];
#pragma unroll
  for (int ct = 0; ct < 4; ++ct) acc[ct] = f32x4{0.f, 0.f, 0.f, 0.f};
  const int wb = LDS_F + kh * 32768 + lane * 16;
  u32x4 wf[2][4];
#pragma unroll
  for (int ct = 0; ct < 4; ++ct) wf[0][ct] = *(LAS u32x4*)(lds_at(wb) + ct * 1024);
  SB();
#pragma unroll
  for (int s = 0; s < 8; ++s) {
    if (s + 1 < 8) {
#pragma unroll
      for (int ct = 0; ct < 4; ++ct) wf[(s + 1) & 1][ct] = *(LAS u32x4*)(lds_at(wb) + (s + 1) * 4096 + ct * 1024);
    }
#pragma unroll
    for (int ct = 0; ct < 4; ++ct) acc[ct] = mfma32(wf[s & 1][ct], of[s], acc[ct]);
    SB();
  }
  STAMP(4 + L * 12 + 9);
  const int red = LDS_W + (12 + tt) * 6144 + lane * 16;
  if (kh) {
#pragma unroll
    for (int ct = 0; ct < 4; ++ct) *(LAS f32x4*)(lds_at(red) + ct * 1024) = acc[ct];
  }
  BAR();
  const int nb = gj & 7;
  if (!kh) {
#pragma unroll
    for (int ct = 0; ct < 4; ++ct) res[ct] += acc[ct] + *(LAS f32x4*)(lds_at(red) + ct * 1024);
    if (L < 3) {
      store_x(x, lane, (L + 1) & 1, res, tau, nb);
      DRAIN();
    } else {
      const int tok = 32 * (tau >> 1) + 2 * c + (tau & 1);
#pragma unroll
      for (int ct = 0; ct < 4; ++ct)
        *(GAS u32x2*)(out + tok * 512 + nb * 64 + ct * 16 + 4 * g) = u32x2{pk2(res[ct].x, res[ct].y), pk2(res[ct].z, res[ct].w)};
    }
  } else if (L < 3) {
    dma_wblk(x, Wnext, 3 + w);
    relayout_wblk(x, w - 1);
  }
  STAMP(4 + L * 12 + 10);
  if (L < 3) {
    BAR();
    set_flag(x, EDGE_X + L + 1, gi, gj);
    if (kh) {
      dma_wblk(x, Wnext, 8 + w);
      asm volatile("s_waitcnt vmcnt(6)" ::: "memory");
      relayout_wblk(x, 3 + w);
      DRAIN();
      relayout_wblk(x, 8 + w);
    } else if (w > 0) {
      if (w == 1) dma_wblk(x, Wnext, 11);
      relayout_wblk(x, w - 1);
      if (w == 1) { DRAIN(); relayout_wblk(x, 11); }
    }
    if (L == 0) {
      BAR();
      csB = colsum(x);
    }
  }
}

__global__ void __launch_bounds__(512, 2)
fused_fwd(const u16* __restrict__ x0, const u16* __restrict__ wA, const u16* __restrict__ wphi, const u16* __restrict__ woA,
          const u16* __restrict__ wB, const u16* __restrict__ woB, u16* __restrict__ out, unsigned char* __restrict__ wsp) {
  extern __shared__ __attribute__((aligned(16))) unsigned char smem[];
  Ctx x;
  x.lane = threadIdx.x & 63;
  x.wave = __builtin_amdgcn_readfirstlane(threadIdx.x >> 6);
  const int b = blockIdx.x;
  {
    constexpr int NJ = MAP_NJ, NI = 32 / NJ, XJ = 16 / NJ;
    const int xc = b & 7, r = b >> 3;
    x.gi = (xc / XJ) * NI + r / NJ;
    x.gj = (xc % XJ) * NJ + r % NJ;
  }
  x.rs = __builtin_amdgcn_make_buffer_rsrc((void*)wsp, 0, WS_END, 0x00020000);
  x.flags = (gu64*)(wsp + WS_FLAGS);
  STAMP(0);
  CSTAMP(60);
  const int lane = x.lane, w = x.wave, c = lane & 15, g = lane >> 4;
  {
    unsigned wv[3];
#pragma unroll
    for (int k = 0; k < 3; ++k) {
      const int ci = k * 64 + lane, row = ci / 12, pc = ci % 12, lc = pc ^ (((row >> 2) & 1) << 1);
      wv[k] = row * 3072 + (lc >> 2) * 1024 + x.gj * 64 + (lc & 3) * 16;
    }
    x.wv0 = wv[0]; x.wv1 = wv[1]; x.wv2 = wv[2];
  }
  f32x4 res[4];
  unsigned long long epoch = 0;
  if (w < 4) {
    epoch = __hip_atomic_load((gu64*)wsp, RLX_AGENT);
    const int tau = 8 * x.gi + 4 * (x.gj >> 3) + w, nb = x.gj & 7;
    const int tok = 32 * (tau >> 1) + 2 * c + (tau & 1);
#pragma unroll
    for (int ct = 0; ct < 4; ++ct) {
      const u32x2 v = *(const GAS u32x2*)(x0 + tok * 512 + nb * 64 + ct * 16 + 4 * g);
      res[ct] = f32x4{bflo(v.x), bfhi(v.x), bflo(v.y), bfhi(v.y)};
    }
    store_x(x, lane, 0, res, tau, nb);
    DRAIN();
  } else {
#pragma unroll
    for (int ct = 0; ct < 4; ++ct) res[ct] = f32x4{0.f, 0.f, 0.f, 0.f};
#pragma unroll 1
    for (int s = w - 4; s < 16; s += 4) dma_wblk(x, wA, s);
  }
  x.tag = __builtin_amdgcn_readfirstlane((unsigned)epoch) | ((unsigned long long)__builtin_amdgcn_readfirstlane((unsigned)(epoch >> 32)) << 32);
  x.tag += 1;
  STAMP(1);
  BAR();
  set_flag(x, EDGE_X + 0, x.gi, x.gj);
  dma_wo(x, woA, LDS_F + w * 8192, LDS_F + w * 8192 + 4096);
  u32x2 wp[2];
#pragma unroll
  for (int h = 0; h < 2; ++h) {
    const GAS u16* pw = (const GAS u16*)wphi + ((2 * x.gj + h) * 16 + 4 * g) * 16 + c;
    const unsigned a0 = pw[0], a1 = pw[16], a2 = pw[32], a3 = pw[48];
    wp[h] = u32x2{a0 | (a1 << 16), a2 | (a3 << 16)};
  }
  DRAIN();
  BAR();
  STAMP(2);
  u32x4 poA[8], poB[8];
  relayout_wblk(x, w);
  relayout_wblk(x, w + 8);
  fetch_wo(x, LDS_F + w * 8192, LDS_F + w * 8192 + 4096, poA);
#pragma unroll
  for (int n = 0; n < 8; ++n) poB[n] = u32x4{0u, 0u, 0u, 0u};
  BAR();
  const f32x4 csA = colsum(x);
  f32x4 csB = {0.f, 0.f, 0.f, 0.f};
#pragma unroll 1
  for (int it = 0; it < 2; ++it) {
    qkv_phase<false>(x, 2 * it, wp, csA, poA, poB, woB, wB, wsp);
    oproj_phase(x, 2 * it, res, csB, wB, out, wsp);
    if (it == 0 && b == 0 && threadIdx.x == 0) __hip_atomic_store((gu64*)wsp, x.tag, RLX_AGENT);
    qkv_phase<true>(x, 2 * it + 1, wp, csB, poB, poA, nullptr, it == 0 ? wA : nullptr, wsp);
    oproj_phase(x, 2 * it + 1, res, csB, wA, out, wsp);
  }
  STAMP(3);
  CSTAMP(61);
}

extern "C" void kernel_launch(void* const* d_in, const int* in_sizes, int n_in, void* d_out, int out_size, void* d_ws, size_t ws_size,
                              hipStream_t stream) {
  (void)in_sizes; (void)n_in; (void)out_size; (void)ws_size;
  (void)hipFuncSetAttribute((const void*)fused_fwd, hipFuncAttributeMaxDynamicSharedMemorySize, LDS_BYTES);
  fused_fwd<<<256, 512, LDS_BYTES, stream>>>((const u16*)d_in[0], (const u16*)d_in[1], (const u16*)d_in[2], (const u16*)d_in[3],
                                             (const u16*)d_in[5], (const u16*)d_in[6], (u16*)d_out, (unsigned char*)d_ws);
}
```

```cpp
#include <hip/hip_runtime.h>
#include <stdint.h>

typedef unsigned short u16;
typedef __bf16 bf16x8 __attribute__((ext_vector_type(8)));
typedef __bf16 bf16x2 __attribute__((ext_vector_type(2)));
typedef short s16x4 __attribute__((ext_vector_type(4)));
typedef float f32x4 __attribute__((ext_vector_type(4)));
typedef float f32x2 __attribute__((ext_vector_type(2)));
typedef unsigned u32x2 __attribute__((ext_vector_type(2)));
typedef unsigned u32x4 __attribute__((ext_vector_type(4)));
#define LAS __attribute__((address_space(3)))
#define GAS __attribute__((address_space(1)))
typedef GAS unsigned gu32;
#define RLX_AGENT __ATOMIC_RELAXED, __HIP_MEMORY_SCOPE_AGENT
#define DI __device__ __forceinline__

constexpr int WS_FLAGS = 0;
constexpr int EDGE_X = 0, EDGE_O = 4;
constexpr int WS_XS = 65536;
constexpr int XS_BUF = 128 * 1024;
constexpr int WS_KZ = WS_XS + 2 * XS_BUF;
constexpr int KZ_BUF = 16 * 4 * 1024;
constexpr int WS_CS = WS_KZ + 2 * KZ_BUF;
constexpr int FRAG_BUF = 2 << 20;
constexpr int WS_XF = 1 << 20;
constexpr int WS_OF = WS_XF + 2 * FRAG_BUF;
constexpr int WS_KV = WS_OF + FRAG_BUF;
constexpr int KV_BUF = 16 * 16 * 4 * 1024;
constexpr int WS_WT = WS_KV + 2 * KV_BUF;
constexpr int WS_WOT = WS_WT + 2 * 16 * 98304;
constexpr int WS_END = WS_WOT + 2 * 8 * 65536;
constexpr int WS_STAMP = 16 << 20;

constexpr int LDS_W = 0;
constexpr int LDS_F = 98304;
constexpr int LDS_CS = LDS_F;
constexpr int LDS_HF = LDS_F + 384;
constexpr int LDS_S = LDS_F + 512;
constexpr int LDS_BYTES = 163840;
constexpr int AUX_SC1 = 16;
#define MAP_NJ 8
constexpr unsigned SPIN_CAP = 1u << 16;

DI unsigned pk2(float a, float b) { f32x2 v = {a, b}; return __builtin_bit_cast(unsigned, __builtin_convertvector(v, bf16x2)); }
DI float bflo(unsigned u) { return __uint_as_float(u << 16); }
DI float bfhi(unsigned u) { return __uint_as_float(u & 0xffff0000u); }
DI f32x4 mfma32(u32x4 a, u32x4 b, f32x4 c) {
  return __builtin_amdgcn_mfma_f32_16x16x32_bf16(__builtin_bit_cast(bf16x8, a), __builtin_bit_cast(bf16x8, b), c, 0, 0, 0);
}
DI f32x4 mfma16(u32x2 a, u32x2 b, f32x4 c) {
  return __builtin_amdgcn_mfma_f32_16x16x16bf16_1k(__builtin_bit_cast(s16x4, a), __builtin_bit_cast(s16x4, b), c, 0, 0, 0);
}
DI LAS unsigned char* lds_at(int off) { return (LAS unsigned char*)(uintptr_t)(unsigned)off; }
DI u32x2 tr_read(LAS unsigned char* p) { return __builtin_bit_cast(u32x2, __builtin_amdgcn_ds_read_tr16_b64_v4i16((LAS s16x4*)p)); }
DI u32x4 tr_frag(LAS unsigned char* p, int second) {
  u32x2 a = tr_read(p), b = tr_read(p + second);
  return u32x4{a.x, a.y, b.x, b.y};
}
DI void swap32(unsigned& a, unsigned& b) { auto r = __builtin_amdgcn_permlane32_swap(a, b, false, false); a = r[0]; b = r[1]; }
DI void swap16(unsigned& a, unsigned& b) { auto r = __builtin_amdgcn_permlane16_swap(a, b, false, false); a = r[0]; b = r[1]; }
DI void swap32f(float& a, float& b) { unsigned x = __float_as_uint(a), y = __float_as_uint(b); swap32(x, y); a = __uint_as_float(x); b = __uint_as_float(y); }
DI void swap16f(float& a, float& b) { unsigned x = __float_as_uint(a), y = __float_as_uint(b); swap16(x, y); a = __uint_as_float(x); b = __uint_as_float(y); }
DI float max3(float a, float b, float c) { return __builtin_fmaxf(__builtin_fmaxf(a, b), c); }
DI void xsum2(float& s1, float& s2) {
  swap32f(s1, s2);
  float t = s1 + s2, u = t;
  swap16f(t, u);
  float a = t + u, b = a;
  swap32f(a, b);
  s1 = a; s2 = b;
}
#define BAR() asm volatile("s_waitcnt lgkmcnt(0)\n\ts_barrier" ::: "memory")
#define DRAIN() asm volatile("s_waitcnt vmcnt(0)" ::: "memory")
#define SB() __builtin_amdgcn_sched_barrier(0)
#define OPAQUE_V(x) asm volatile("" : "+v"(x))
#define OPAQUE_S(x) asm volatile("" : "+s"(x))

DI void dma16(const void* sbase, unsigned voff, unsigned lds_addr) {
  unsigned keep;
  asm volatile("s_mov_b32 %0, m0\n\ts_mov_b32 m0, %3\n\ts_nop 2\n\tglobal_load_lds_dwordx4 %1, %2\n\ts_mov_b32 m0, %0"
               : "=&s"(keep) : "v"(voff), "s"(sbase), "s"(lds_addr) : "memory");
}

struct Ctx {
  __amdgpu_buffer_rsrc_t rs;
  int lane, wave, gi, gj;
};

DI u32x4 ld128(const Ctx& x, int voff, int soff) { return __builtin_amdgcn_raw_buffer_load_b128(x.rs, voff, soff, AUX_SC1); }
DI u32x4 ldw128(const Ctx& x, int voff, int soff) { return __builtin_amdgcn_raw_buffer_load_b128(x.rs, voff, soff, 0); }
DI void st128(const Ctx& x, u32x4 v, int voff, int soff) { __builtin_amdgcn_raw_buffer_store_b128(v, x.rs, voff + soff, 0, AUX_SC1); }
DI void st64(const Ctx& x, u32x2 v, int voff, int soff) { __builtin_amdgcn_raw_buffer_store_b64(v, x.rs, voff, soff, AUX_SC1); }
DI void st32(const Ctx& x, unsigned v, int voff, int soff) { __builtin_amdgcn_raw_buffer_store_b32(v, x.rs, voff, soff, AUX_SC1); }

#define STAMP(n) do {} while (0)
#define STAMP_FLUSH() do {} while (0)

DI void signal(const Ctx& x, int lane, int word) {
  DRAIN();
  if (lane == 0) st32(x, 1u, 0, WS_FLAGS + word * 4);
}
DI void poll(const Ctx& x, int voff) {
  for (unsigned spins = 0; spins < SPIN_CAP; ++spins) {
    asm volatile("" ::: "memory");
    const unsigned v = __builtin_amdgcn_raw_buffer_load_b32(x.rs, voff, WS_FLAGS, AUX_SC1);
    if (__all(v != 0u)) break;
  }
  __builtin_amdgcn_fence(__ATOMIC_ACQUIRE, "wavefront");
  asm volatile("" ::: "memory");
}

DI void store_x(const Ctx& x, int lane, int buf, const f32x4 (&r)[4], int tau, int nb) {
#pragma unroll
  for (int a = 0; a < 2; ++a) {
    u32x4 f = {pk2(r[2 * a].x, r[2 * a].y), pk2(r[2 * a].z, r[2 * a].w), pk2(r[2 * a + 1].x, r[2 * a + 1].y), pk2(r[2 * a + 1].z, r[2 * a + 1].w)};
    st128(x, f, lane * 16 + a * 1024, WS_XF + buf * FRAG_BUF + (tau * 16 + nb * 2) * 1024);
  }
  const f32x4 s = (r[0] + r[1]) + (r[2] + r[3]);
  const f32x4 q = (r[0] * r[0] + r[1] * r[1]) + (r[2] * r[2] + r[3] * r[3]);
  float s1 = (s.x + s.y) + (s.z + s.w), s2 = (q.x + q.y) + (q.z + q.w);
  xsum2(s1, s2);
  if (lane < 16) st64(x, u32x2{__float_as_uint(s1), __float_as_uint(s2)}, lane * 16, WS_XS + buf * XS_BUF + tau * 1024 + (nb >> 1) * 256 + (nb & 1) * 8);
}
DI void load_x0(const u16* x0, int lane, int tau, int nb, f32x4 (&r)[4]) {
  const int tok = 32 * (tau >> 1) + 2 * (lane & 15) + (tau & 1);
#pragma unroll
  for (int ct = 0; ct < 4; ++ct) {
    const u32x2 v = *(const GAS u32x2*)(x0 + tok * 512 + nb * 64 + ct * 16 + 4 * (lane >> 4));
    r[ct] = f32x4{bflo(v.x), bfhi(v.x), bflo(v.y), bfhi(v.y)};
  }
}

DI void finish_stats(u32x4 p, float& mean, float& rstd) {
  float s1 = __uint_as_float(p.x) + __uint_as_float(p.z), s2 = __uint_as_float(p.y) + __uint_as_float(p.w);
  xsum2(s1, s2);
  mean = s1 * (1.f / 512.f);
  float var = fmaxf(s2 * (1.f / 512.f) - mean * mean, 0.f);
  rstd = __builtin_amdgcn_rsqf(var + 1e-5f);
}
DI u32x2 ln_pack(f32x4 a, f32x4 cs, float mean, float rstd) {
  float v0 = fmaf(-mean, cs.x, a.x) * rstd, v1 = fmaf(-mean, cs.y, a.y) * rstd;
  float v2 = fmaf(-mean, cs.z, a.z) * rstd, v3 = fmaf(-mean, cs.w, a.w) * rstd;
  return u32x2{pk2(v0, v1), pk2(v2, v3)};
}

DI void feature_map(const u32x2 (&in)[2], const u32x2 (&wp)[2], unsigned (&o)[8]) {
  f32x4 p[4];
  const f32x4 z = {0.f, 0.f, 0.f, 0.f};
#pragma unroll
  for (int h = 0; h < 2; ++h) {
    p[2 * h] = mfma16(wp[h], in[h], z);
    p[2 * h + 1] = mfma16(u32x2{wp[h].x ^ 0x80008000u, wp[h].y ^ 0x80008000u}, in[h], z);
  }
  float v[16];
#pragma unroll
  for (int i = 0; i < 4; ++i)
#pragma unroll
    for (int r = 0; r < 4; ++r) v[4 * i + r] = p[i][r];
#pragma unroll
  for (int r = 0; r < 4; ++r) { swap32f(v[r], v[8 + r]); swap32f(v[4 + r], v[12 + r]); }
#pragma unroll
  for (int r = 0; r < 4; ++r) { swap16f(v[r], v[4 + r]); swap16f(v[8 + r], v[12 + r]); }
  const float m = max3(max3(max3(v[0], v[1], v[2]), max3(v[3], v[4], v[5]), max3(v[6], v[7], v[8])),
                       max3(max3(v[9], v[10], v[11]), max3(v[12], v[13], v[14]), v[15]), v[15]);
  const float LOG2E = 1.4426950408889634f;
  const float mm = -m * LOG2E;
#pragma unroll
  for (int i = 0; i < 16; ++i) v[i] = __builtin_amdgcn_exp2f(fmaf(v[i], LOG2E, mm));
  f32x2 s2 = {0.f, 0.f};
#pragma unroll
  for (int i = 0; i < 16; i += 4) s2 += (f32x2{v[i], v[i + 1]} + f32x2{v[i + 2], v[i + 3]});
  const float rs = __builtin_amdgcn_rcpf(s2.x + s2.y);
#pragma unroll
  for (int i = 0; i < 8; ++i) { const f32x2 t = f32x2{v[2 * i], v[2 * i + 1]} * rs; o[i] = pk2(t.x, t.y); }
}

#define XWIN 6
constexpr int XW = XWIN;
#define LDX(s) ld128(x, lane * 16 + ((s) & 3) * 1024, xbase + ((s) >> 2) * 4096)

template <bool IS_B>
DI void qkv_phase(const Ctx& x, int L, const u32x2 (&wp)[2], f32x4 cskeep, u32x4 (&wn)[12], unsigned char* wsp) {
  int lane = x.lane, w = x.wave, gi = x.gi, gj = x.gj;
  OPAQUE_V(lane); OPAQUE_S(w); OPAQUE_S(gi); OPAQUE_S(gj);
  const int c = lane & 15, g = lane >> 4;
  const int q = c >> 2, p = c & 3;
  if (w >= 1 && w <= 6 && c == 0) *(LAS f32x4*)lds_at(LDS_CS + (w - 1) * 64 + g * 16) = cskeep;
  if (IS_B && w == 7 && lane < 8) *(LAS unsigned*)lds_at(LDS_HF + lane * 4) = 0u;
  BAR();
  STAMP(4 + L * 12 + 0);
  constexpr int XB = IS_B ? 1 : 0;
  const int tau = 8 * gi + w;
  const bool has_halo = IS_B && gi > 0;
  if (IS_B || L > 0) {
    int fo = ((EDGE_X + L) * 128 + tau) * 64 + (lane & 7) * 4;
    if (has_halo && lane >= 8 && lane < 24) fo = ((EDGE_X + L) * 128 + 8 * gi - 2 + ((lane >> 3) & 1)) * 64 + (w < 2 ? (lane & 7) : w) * 4;
    poll(x, fo);
  }
  STAMP(4 + L * 12 + 1);
  const int xbase = WS_XF + XB * FRAG_BUF + tau * 16384;
  u32x4 hx[2], hsp;
  const int ht = 8 * gi - 2 + (c >> 3), hc = 8 + (c & 7);
  if (IS_B) {
#pragma unroll
    for (int s = 0; s < 2; ++s) hx[s] = ld128(x, has_halo ? ht * 16384 + (g * 16 + hc) * 16 + s * 1024 : 0x7ffffff0, WS_XF + XB * FRAG_BUF + w * 2048);
  }
  u32x4 xf[16];
#pragma unroll
  for (int s = 0; s < XW; ++s) xf[s] = LDX(s);
  const u32x4 sp = ld128(x, lane * 16, WS_XS + XB * XS_BUF + tau * 1024);
  constexpr int KIM = LDS_S, VIMB = KIM + 8192, HP = VIMB + 10240;
  if (IS_B) {
    if (w < 2) hsp = ld128(x, has_halo ? (8 * gi - 2 + w) * 1024 + (g * 16 + hc) * 16 : 0x7ffffff0, WS_XS + XB * XS_BUF);
    u32x4 f[8];
#pragma unroll
    for (int i = 0; i < 8; ++i) f[i] = *(LAS u32x4*)(lds_at(LDS_W + w * 12288 + 2048 + lane * 16) + (i >> 2) * 6144 + (i & 3) * 1024);
#pragma unroll
    for (int i = 0; i < 4; ++i) {
      f32x4 h = mfma32(f[i], hx[0], f32x4{0.f, 0.f, 0.f, 0.f});
      h = mfma32(f[4 + i], hx[1], h);
      *(LAS f32x4*)(lds_at(HP + w * 4096 + lane * 16) + i * 1024) = h;
    }
    asm volatile("s_waitcnt lgkmcnt(0)" ::: "memory");
    if (lane == 0) *(volatile LAS unsigned*)lds_at(LDS_HF + w * 4) = 1u;
  }
  int wa0 = LDS_W + lane * 16, wa1 = LDS_W + 49152 + lane * 16;
  OPAQUE_V(wa1);
  constexpr int C0 = IS_B ? 0 : 2;
  constexpr int NC = 6 - C0;
  f32x4 acc[6];
#pragma unroll
  for (int ct = 0; ct < 6; ++ct) acc[ct] = f32x4{0.f, 0.f, 0.f, 0.f};
  u32x4 wf[2][6];
#pragma unroll
  for (int ct = C0; ct < 6; ++ct) wf[0][ct] = *(LAS u32x4*)(lds_at(wa0) + ct * 1024);
  float mean, rstd;
  u32x4 po[8];
  const int wnbase = L < 3 ? WS_WT + ((IS_B ? 0 : 16) + gj) * 98304 + w * 12288 : 0x7ff00000;
  const int pobase = WS_WOT + ((IS_B ? 8 : 0) + (gj & 7)) * 65536 + w * 8192;
  SB();
#pragma unroll
  for (int s = 0; s < 16; ++s) {
    if (s + XW < 16) xf[s + XW] = LDX(s + XW);
    if (IS_B && s >= 10) {
      if (s < 12) {
#pragma unroll
        for (int n = 0; n < 4; ++n) po[(s - 10) * 4 + n] = ldw128(x, lane * 16 + n * 1024, pobase + (s - 10) * 4096);
      } else if (s < 15) {
#pragma unroll
        for (int n = 0; n < 4; ++n) wn[(s - 12) * 4 + n] = ldw128(x, lane * 16 + n * 1024, wnbase + (s - 12) * 4096);
      }
    }
    if (s + 1 < 16) {
#pragma unroll
      for (int ct = C0; ct < 6; ++ct) wf[(s + 1) & 1][ct] = *(LAS u32x4*)(lds_at(s + 1 < 8 ? wa0 : wa1) + ((s + 1) & 7) * 6144 + ct * 1024);
    }
#pragma unroll
    for (int ct = C0; ct < 6; ++ct) acc[ct] = mfma32(wf[s & 1][ct], xf[s], acc[ct]);
    if (s == 9) finish_stats(sp, mean, rstd);
    if (s + 1 < 16) {
#pragma unroll
      for (int i = 0; i < NC; ++i) {
        __builtin_amdgcn_sched_group_barrier(0x008, 1, 0);
        __builtin_amdgcn_sched_group_barrier(0x100, 1, 0);
        if (s == 9) __builtin_amdgcn_sched_group_barrier(0x002, 5, 0);
        if (IS_B && s >= 10) __builtin_amdgcn_sched_group_barrier(0x020, 1, 0);
      }
    }
    SB();
  }
  f32x4 cs[6];
#pragma unroll
  for (int ct = C0; ct < 6; ++ct) cs[ct] = *(LAS f32x4*)(lds_at(LDS_CS + g * 16) + ct * 64);
  STAMP(4 + L * 12 + 2);
  const int obase = WS_OF + tau * 16384 + gj * 1024;
  if (!IS_B) {
    constexpr int PHI = LDS_S, VIM = PHI + 16384, KVOP = VIM + 8192, ZOP = KVOP + 2048;
#pragma unroll
    for (int n = 0; n < 12; ++n) wn[n] = ldw128(x, lane * 16 + (n & 3) * 1024, wnbase + (n >> 2) * 4096);
    SB();
    {
      const int row = w * 16 + c;
      u32x2 kn[2];
#pragma unroll
      for (int h = 0; h < 2; ++h) kn[h] = ln_pack(acc[2 + h], cs[2 + h], mean, rstd);
      unsigned ph[8];
      feature_map(kn, wp, ph);
#pragma unroll
      for (int h = 0; h < 2; ++h) *(LAS u32x2*)lds_at(VIM + h * 4096 + row * 32 + g * 8) = ln_pack(acc[4 + h], cs[4 + h], mean, rstd);
      *(LAS u32x4*)lds_at(PHI + g * 4096 + row * 32) = u32x4{ph[0], ph[1], ph[2], ph[3]};
      *(LAS u32x4*)lds_at(PHI + g * 4096 + row * 32 + 16) = u32x4{ph[4], ph[5], ph[6], ph[7]};
    }
    BAR();
    const int e = L >> 1;
    if (w < 4) {
      const u32x4 ones = {0x3f803f80u, 0x3f803f80u, 0x3f803f80u, 0x3f803f80u};
      f32x4 kv0 = {0.f, 0.f, 0.f, 0.f}, kv1 = kv0, z0 = kv0, z1 = kv0;
      LAS unsigned char* pa = lds_at(PHI + w * 4096 + (4 * g + q) * 32 + p * 8);
      LAS unsigned char* pb = lds_at(VIM + (w >> 1) * 4096 + (4 * g + q) * 32 + p * 8);
#pragma unroll
      for (int u = 0; u < 4; u += 2) {
        const u32x4 a0 = tr_frag(pa + u * 1024, 512), b0 = tr_frag(pb + u * 1024, 512);
        const u32x4 a1 = tr_frag(pa + u * 1024 + 1024, 512), b1 = tr_frag(pb + u * 1024 + 1024, 512);
        kv0 = mfma32(a0, b0, kv0); kv1 = mfma32(a1, b1, kv1);
        z0 = mfma32(a0, ones, z0); z1 = mfma32(a1, ones, z1);
      }
      kv0 += kv1; z0 += z1;
      st128(x, __builtin_bit_cast(u32x4, kv0), lane * 16, WS_KV + e * KV_BUF + ((gj * 16 + gi) * 4 + w) * 1024);
      const float zv = c == 0 ? z0.x : c == 1 ? z0.y : c == 2 ? z0.z : z0.w;
      if (c < 4) st32(x, __float_as_uint(zv), (4 * g + c) * 64, WS_KZ + e * KZ_BUF + (gj * 4 + w) * 1024 + gi * 4);
    }
    STAMP(4 + L * 12 + 3);
    u32x4 pq[2];
    {
#pragma unroll
      for (int s = 0; s < 16; s += 2) {
        u32x4 f[4];
#pragma unroll
        for (int i = 0; i < 4; ++i) f[i] = *(LAS u32x4*)(lds_at(s < 8 ? wa0 : wa1) + ((s + (i >> 1)) & 7) * 6144 + (i & 1) * 1024);
#pragma unroll
        for (int i = 0; i < 4; ++i) acc[i & 1] = mfma32(f[i], xf[s + (i >> 1)], acc[i & 1]);
        if (s == 6) {
          SB();
          if (w < 4) signal(x, lane, (e * 64 + gj * 4 + w) * 16 + gi);
#pragma unroll
          for (int n = 0; n < 8; ++n) po[n] = ldw128(x, lane * 16 + (n & 3) * 1024, pobase + (n >> 2) * 4096);
          SB();
        }
      }
      u32x2 qn[2];
#pragma unroll
      for (int h = 0; h < 2; ++h) qn[h] = ln_pack(acc[h], *(LAS f32x4*)(lds_at(LDS_CS + g * 16) + h * 64), mean, rstd);
      unsigned ph[8];
      feature_map(qn, wp, ph);
#pragma unroll
      for (int r = 0; r < 2; ++r) { swap16(ph[r], ph[2 + r]); swap16(ph[4 + r], ph[6 + r]); }
#pragma unroll
      for (int r = 0; r < 2; ++r) { swap32(ph[r], ph[4 + r]); swap32(ph[2 + r], ph[6 + r]); }
      pq[0] = u32x4{ph[0], ph[1], ph[2], ph[3]};
      pq[1] = u32x4{ph[4], ph[5], ph[6], ph[7]};
    }
    STAMP(4 + L * 12 + 4);
    poll(x, (e * 64 + gj * 4 + (w & 3)) * 64 + c * 4);
    STAMP(4 + L * 12 + 5);
    if (w < 4) {
      const int sbase = WS_KV + e * KV_BUF + (gj * 64 + w) * 1024;
      u32x4 a[16];
#pragma unroll
      for (int i = 0; i < 16; ++i) a[i] = ld128(x, lane * 16, sbase + i * 4096);
      SB();
      f32x4 t[4];
#pragma unroll
      for (int i = 0; i < 4; ++i)
        t[i] = (__builtin_bit_cast(f32x4, a[4 * i]) + __builtin_bit_cast(f32x4, a[4 * i + 1])) + (__builtin_bit_cast(f32x4, a[4 * i + 2]) + __builtin_bit_cast(f32x4, a[4 * i + 3]));
      const f32x4 kv = (t[0] + t[1]) + (t[2] + t[3]);
      *(LAS u32x2*)lds_at(KVOP + (w >> 1) * 1024 + lane * 16 + (w & 1) * 8) = u32x2{pk2(kv.x, kv.y), pk2(kv.z, kv.w)};
    } else {
      const int wi = w - 4;
      const f32x4 a = __builtin_bit_cast(f32x4, ld128(x, lane * 16, WS_KZ + e * KZ_BUF + (gj * 4 + wi) * 1024));
      float s = (a.x + a.y) + (a.z + a.w);
      s += __builtin_bit_cast(float, __builtin_amdgcn_update_dpp(0, __builtin_bit_cast(int, s), 0xB1, 0xf, 0xf, true));
      s += __builtin_bit_cast(float, __builtin_amdgcn_update_dpp(0, __builtin_bit_cast(int, s), 0x4E, 0xf, 0xf, true));
      const int f = lane >> 2;
      if ((lane & 3) == 0) *(LAS u16*)lds_at(ZOP + (wi >> 1) * 64 + (f >> 2) * 16 + (wi & 1) * 8 + (f & 3) * 2) = (u16)pk2(s, s);
    }
    BAR();
    {
      const f32x4 z = {0.f, 0.f, 0.f, 0.f};
      unsigned o[4];
#pragma unroll
      for (int h = 0; h < 2; ++h) {
        const u32x4 kvop = *(LAS u32x4*)lds_at(KVOP + h * 1024 + lane * 16);
        const u32x4 zop = *(LAS u32x4*)lds_at(ZOP + h * 64 + g * 16);
        f32x4 num = mfma32(kvop, pq[h], z), den = mfma32(zop, pq[h], z);
        const float r = __builtin_amdgcn_rcpf(den.x + 1e-6f);
        o[2 * h] = pk2(num.x * r, num.y * r); o[2 * h + 1] = pk2(num.z * r, num.w * r);
      }
      st128(x, u32x4{o[0], o[1], o[2], o[3]}, lane * 16, obase);
    }
  } else {
    constexpr int VIM = VIMB;
    u32x2 qn[2], kn[2], kp[2];
#pragma unroll
    for (int h = 0; h < 2; ++h) {
      kn[h] = ln_pack(acc[2 + h], cs[2 + h], mean, rstd);
      *(LAS u32x2*)lds_at(KIM + (w * 2 + h) * 512 + lane * 8) = kn[h];
      *(LAS u32x2*)lds_at(VIM + ((2 + w) * 2 + h) * 512 + c * 32 + g * 8) = ln_pack(acc[4 + h], cs[4 + h], mean, rstd);
    }
    if (w < 2) {
      for (unsigned spins = 0; spins < SPIN_CAP; ++spins) {
        const unsigned v = *(volatile LAS unsigned*)lds_at(LDS_HF + (lane & 7) * 4);
        if (__all(v != 0u)) break;
      }
      float hm, hr;
      finish_stats(hsp, hm, hr);
      const int src = HP + (c < 8 ? 2048 : 0) + (g * 16 + w * 8 + (c & 7)) * 16;
#pragma unroll
      for (int h = 0; h < 2; ++h) {
        f32x4 a[8];
#pragma unroll
        for (int i = 0; i < 8; ++i) a[i] = *(LAS f32x4*)(lds_at(src) + i * 4096 + h * 1024);
        const f32x4 sum = ((a[0] + a[1]) + (a[2] + a[3])) + ((a[4] + a[5]) + (a[6] + a[7]));
        const f32x4 csw = c < 8 ? cs[4 + h] : cs[2 + h];
        u32x2 v = ln_pack(sum, csw, hm, hr);
        if (!has_halo) v = u32x2{0u, 0u};
        kp[h] = c < 8 ? u32x2{0u, 0u} : v;
        *(LAS u32x2*)lds_at(VIM + (w * 2 + h) * 512 + (c ^ 8) * 32 + g * 8) = c < 8 ? v : u32x2{0u, 0u};
      }
    }
#pragma unroll
    for (int h = 0; h < 2; ++h) qn[h] = ln_pack(acc[h], cs[h], mean, rstd);
    const float NEG = -1e30f;
    f32x4 bo, bp;
    const bool noprev = (w < 2) && !has_halo;
#pragma unroll
    for (int r = 0; r < 4; ++r) {
      const int key = 4 * g + r;
      bo[r] = (c - key >= 0 && c - key <= 7) ? 0.f : NEG;
      bp[r] = (key >= c + 9 && !noprev) ? 0.f : NEG;
    }
    f32x4 so[2];
#pragma unroll
    for (int h = 0; h < 2; ++h) so[h] = mfma16(kn[h], qn[h], bo);
    BAR();
    {
      const u32x4 ones = {0x3f803f80u, 0x3f803f80u, 0x3f803f80u, 0x3f803f80u};
      const f32x4 z = {0.f, 0.f, 0.f, 0.f};
      const float SC = 0.25f * 1.4426950408889634f;
      f32x4 sp_[2];
      u32x2 vo[2], vp[2];
      float m[2];
#pragma unroll
      for (int h = 0; h < 2; ++h) {
        if (w >= 2) kp[h] = *(LAS u32x2*)lds_at(KIM + ((w - 2) * 2 + h) * 512 + lane * 8);
        vp[h] = tr_read(lds_at(VIM + (w * 2 + h) * 512 + (4 * g + q) * 32 + p * 8));
        vo[h] = tr_read(lds_at(VIM + ((2 + w) * 2 + h) * 512 + (4 * g + q) * 32 + p * 8));
      }
#pragma unroll
      for (int h = 0; h < 2; ++h) {
        sp_[h] = mfma16(kp[h], qn[h], bp);
        m[h] = max3(max3(so[h].x, so[h].y, so[h].z), max3(so[h].w, sp_[h].x, sp_[h].y), max3(sp_[h].z, sp_[h].w, sp_[h].w));
      }
      swap32f(m[0], m[1]);
      float t = max3(m[0], m[1], m[1]), u = t;
      swap16f(t, u);
      float a = max3(t, u, u), b = a;
      swap32f(a, b);
      m[0] = a; m[1] = b;
      unsigned o[4];
#pragma unroll
      for (int h = 0; h < 2; ++h) {
        const float mm = -m[h] * SC;
        float e[8];
#pragma unroll
        for (int r = 0; r < 4; ++r) { e[r] = __builtin_amdgcn_exp2f(fmaf(so[h][r], SC, mm)); e[4 + r] = __builtin_amdgcn_exp2f(fmaf(sp_[h][r], SC, mm)); }
        const u32x4 P = {pk2(e[0], e[1]), pk2(e[2], e[3]), pk2(e[4], e[5]), pk2(e[6], e[7])};
        f32x4 num = mfma32(u32x4{vo[h].x, vo[h].y, vp[h].x, vp[h].y}, P, z), den = mfma32(ones, P, z);
        const float r = __builtin_amdgcn_rcpf(den.x);
        o[2 * h] = pk2(num.x * r, num.y * r); o[2 * h + 1] = pk2(num.z * r, num.w * r);
      }
      st128(x, u32x4{o[0], o[1], o[2], o[3]}, lane * 16, obase);
    }
  }
  STAMP(4 + L * 12 + 6);
  signal(x, lane, ((EDGE_O + L) * 128 + tau) * 16 + gj);
  BAR();
#pragma unroll
  for (int n = 0; n < 8; ++n) *(LAS u32x4*)(lds_at(LDS_F + w * 8192 + lane * 16) + n * 1024) = po[n];
}

DI void oproj_phase(const Ctx& x, int L, f32x4 (&res)[4], const u32x4 (&wn)[12], u16* out, unsigned char* wsp) {
  int lane = x.lane, w = x.wave, gi = x.gi, gj = x.gj;
  OPAQUE_V(lane); OPAQUE_S(w); OPAQUE_S(gi); OPAQUE_S(gj);
  const int c = lane & 15, g = lane >> 4;
  const int tt = w & 3, kh = w >> 2;
  const int tau = 8 * gi + 4 * (gj >> 3) + tt;
  BAR();
  STAMP(4 + L * 12 + 7);
  poll(x, ((EDGE_O + L) * 128 + tau) * 64 + (kh * 8 + (lane & 7)) * 4);
  STAMP(4 + L * 12 + 8);
  u32x4 of[8];
  const int obase = WS_OF + tau * 16384 + kh * 8192;
#pragma unroll
  for (int s = 0; s < 8; ++s) of[s] = ld128(x, lane * 16 + (s & 3) * 1024, obase + (s >> 2) * 4096);
  f32x4 acc[4];
#pragma unroll
  for (int ct = 0; ct < 4; ++ct) acc[ct] = f32x4{0.f, 0.f, 0.f, 0.f};
  const int wb = LDS_F + kh * 32768 + lane * 16;
  u32x4 wf[2][4];
#pragma unroll
  for (int ct = 0; ct < 4; ++ct) wf[0][ct] = *(LAS u32x4*)(lds_at(wb) + ct * 1024);
  SB();
#pragma unroll
  for (int s = 0; s < 8; ++s) {
    if (s + 1 < 8) {
#pragma unroll
      for (int ct = 0; ct < 4; ++ct) wf[(s + 1) & 1][ct] = *(LAS u32x4*)(lds_at(wb) + (s + 1) * 4096 + ct * 1024);
    }
#pragma unroll
    for (int ct = 0; ct < 4; ++ct) acc[ct] = mfma32(wf[s & 1][ct], of[s], acc[ct]);
    SB();
  }
  STAMP(4 + L * 12 + 9);
  const int red = LDS_W + tt * 12288 + lane * 16;
  if (kh) {
#pragma unroll
    for (int ct = 0; ct < 4; ++ct) *(LAS f32x4*)(lds_at(red) + ct * 1024) = acc[ct];
  }
  BAR();
  const int nb = gj & 7;
  if (!kh) {
#pragma unroll
    for (int ct = 0; ct < 4; ++ct) res[ct] += acc[ct] + *(LAS f32x4*)(lds_at(red) + ct * 1024);
    if (L < 3) {
      store_x(x, lane, (L + 1) & 1, res, tau, nb);
      signal(x, lane, ((EDGE_X + L + 1) * 128 + tau) * 16 + nb);
    } else {
      const int tok = 32 * (tau >> 1) + 2 * c + (tau & 1);
#pragma unroll
      for (int ct = 0; ct < 4; ++ct)
        *(GAS u32x2*)(out + tok * 512 + nb * 64 + ct * 16 + 4 * g) = u32x2{pk2(res[ct].x, res[ct].y), pk2(res[ct].z, res[ct].w)};
    }
  }
  STAMP(4 + L * 12 + 10);
  if (L < 3) {
#pragma unroll
    for (int n = 0; n < 12; ++n) *(LAS u32x4*)(lds_at(LDS_W + w * 12288 + lane * 16) + n * 1024) = wn[n];
  }
}

DI void map_block(int b, int& gi, int& gj) {
  constexpr int NJ = MAP_NJ, NI = 32 / NJ, XJ = 16 / NJ;
  const int xc = b & 7, r = b >> 3;
  gi = (xc / XJ) * NI + r / NJ;
  gj = (xc % XJ) * NJ + r % NJ;
}

__global__ void __launch_bounds__(512, 2)
fused_fwd(const u16* __restrict__ x0, const u16* __restrict__ wphi, u16* __restrict__ out, unsigned char* __restrict__ wsp) {
  extern __shared__ __attribute__((aligned(16))) unsigned char smem[];
  Ctx x;
  x.lane = threadIdx.x & 63;
  x.wave = __builtin_amdgcn_readfirstlane(threadIdx.x >> 6);
  map_block(blockIdx.x, x.gi, x.gj);
  x.rs = __builtin_amdgcn_make_buffer_rsrc((void*)wsp, 0, WS_END, 0x00020000);
  STAMP(0);
  const int lane = x.lane, w = x.wave, c = lane & 15, g = lane >> 4;
  {
    const unsigned char* src = wsp + WS_WT + x.gj * 98304 + w * 12288;
#pragma unroll
    for (int n = 0; n < 12; ++n) dma16(src + n * 1024, lane * 16, LDS_W + w * 12288 + n * 1024);
  }
  f32x4 csA = {0.f, 0.f, 0.f, 0.f}, csB = csA;
  if (w >= 1 && w <= 6) {
    csA = *(const GAS f32x4*)(wsp + WS_CS + x.gj * 384 + (w - 1) * 64 + g * 16);
    csB = *(const GAS f32x4*)(wsp + WS_CS + (16 + x.gj) * 384 + (w - 1) * 64 + g * 16);
  }
  u32x2 wp[2];
#pragma unroll
  for (int h = 0; h < 2; ++h) {
    const GAS u16* pw = (const GAS u16*)wphi + ((2 * x.gj + h) * 16 + 4 * g) * 16 + c;
    const unsigned a0 = pw[0], a1 = pw[16], a2 = pw[32], a3 = pw[48];
    wp[h] = u32x2{a0 | (a1 << 16), a2 | (a3 << 16)};
  }
  f32x4 res[4];
  if (w < 4) load_x0(x0, lane, 8 * x.gi + 4 * (x.gj >> 3) + w, x.gj & 7, res);
  else {
#pragma unroll
    for (int ct = 0; ct < 4; ++ct) res[ct] = f32x4{0.f, 0.f, 0.f, 0.f};
  }
  DRAIN();
  STAMP(2);
  u32x4 wn[12];
#pragma unroll 1
  for (int it = 0; it < 2; ++it) {
    qkv_phase<false>(x, 2 * it, wp, csA, wn, wsp);
    oproj_phase(x, 2 * it, res, wn, out, wsp);
    qkv_phase<true>(x, 2 * it + 1, wp, csB, wn, wsp);
    oproj_phase(x, 2 * it + 1, res, wn, out, wsp);
  }
  STAMP(3);
  STAMP_FLUSH();
}

__global__ void __launch_bounds__(256)
setup_k(const u16* __restrict__ x0, const u16* __restrict__ wA, const u16* __restrict__ woA, const u16* __restrict__ wB,
        const u16* __restrict__ woB, unsigned char* __restrict__ wsp) {
  __shared__ __attribute__((aligned(16))) unsigned char img[16384 + 256];
  Ctx x;
  const int tid = threadIdx.x, lane = tid & 63, w = __builtin_amdgcn_readfirstlane(tid >> 6), b = blockIdx.x;
  x.lane = lane; x.wave = w;
  x.rs = __builtin_amdgcn_make_buffer_rsrc((void*)wsp, 0, WS_END, 0x00020000);
  const u16* src; int ld, dst, fstr;
  if (b < 192) {
    const int type = b / 96, r = b % 96, gj = r / 6, ct = r % 6;
    src = (type ? wB : wA) + (ct >> 1) * 512 + gj * 32 + (ct & 1) * 16; ld = 1536;
    dst = WS_WT + (type * 16 + gj) * 98304 + ct * 1024; fstr = 6144;
  } else {
    const int u = b - 192, type = u >> 5, nb = (u >> 2) & 7, ct = u & 3;
    src = (type ? woB : woA) + nb * 64 + ct * 16; ld = 512;
    dst = WS_WOT + (type * 8 + nb) * 65536 + ct * 1024; fstr = 4096;
  }
  u32x4 v[4];
#pragma unroll
  for (int i = 0; i < 4; ++i) v[i] = *(const GAS u32x4*)(src + (size_t)((tid >> 1) + 128 * i) * ld + (tid & 1) * 8);
  f32x4 r[4];
  const int tau = (b >> 3) * 4 + w, nb = b & 7;
  load_x0(x0, lane, tau, nb, r);
  if (tid < 16) *(GAS u32x4*)(wsp + WS_FLAGS + b * 256 + tid * 16) = u32x4{0u, 0u, 0u, 0u};
  LAS unsigned char* L = (LAS unsigned char*)img;
#pragma unroll
  for (int i = 0; i < 4; ++i) *(LAS u32x4*)(L + i * 4096 + tid * 16) = v[i];
  store_x(x, lane, 0, r, tau, nb);
  __syncthreads();
  const int c = lane & 15, g = lane >> 4;
  const u32x4 ones = {0x3f803f80u, 0x3f803f80u, 0x3f803f80u, 0x3f803f80u};
  f32x4 cs = {0.f, 0.f, 0.f, 0.f};
#pragma unroll
  for (int i = 0; i < 4; ++i) {
    const int s = 4 * w + i;
    const u32x4 f = tr_frag(L + s * 1024 + (4 * g + (c >> 2)) * 32 + (c & 3) * 8, 512);
    *(GAS u32x4*)(wsp + dst + s * fstr + lane * 16) = f;
    cs = mfma32(f, ones, cs);
  }
  if (b < 192) {
    if (c == 0) *(LAS f32x4*)(L + 16384 + w * 64 + g * 16) = cs;
    __syncthreads();
    if (tid < 16) {
      const LAS float* p = (const LAS float*)(L + 16384) + tid;
      ((GAS float*)(wsp + WS_CS))[b * 16 + tid] = (p[0] + p[16]) + (p[32] + p[48]);
    }
  }
}

extern "C" void kernel_launch(void* const* d_in, const int* in_sizes, int n_in, void* d_out, int out_size, void* d_ws, size_t ws_size,
                              hipStream_t stream) {
  (void)in_sizes; (void)n_in; (void)out_size; (void)ws_size;
  (void)hipFuncSetAttribute((const void*)fused_fwd, hipFuncAttributeMaxDynamicSharedMemorySize, LDS_BYTES);
  setup_k<<<256, 256, 0, stream>>>((const u16*)d_in[0], (const u16*)d_in[1], (const u16*)d_in[3], (const u16*)d_in[5], (const u16*)d_in[6],
                                   (unsigned char*)d_ws);
  fused_fwd<<<256, 512, LDS_BYTES, stream>>>((const u16*)d_in[0], (const u16*)d_in[2], (u16*)d_out, (unsigned char*)d_ws);
}
```

```cpp
#include <hip/hip_runtime.h>
#include <stdint.h>

typedef unsigned short u16;
typedef __bf16 bf16x8 __attribute__((ext_vector_type(8)));
typedef __bf16 bf16x2 __attribute__((ext_vector_type(2)));
typedef short s16x4 __attribute__((ext_vector_type(4)));
typedef float f32x4 __attribute__((ext_vector_type(4)));
typedef float f32x2 __attribute__((ext_vector_type(2)));
typedef unsigned u32x2 __attribute__((ext_vector_type(2)));
typedef unsigned u32x4 __attribute__((ext_vector_type(4)));
#define LAS __attribute__((address_space(3)))
#define GAS __attribute__((address_space(1)))
#define DI __device__ __forceinline__

constexpr int WS_EPOCH = 0;
#define FSTR 8
constexpr int WS_FLAGS = FSTR == 8 ? 4096 : (16 << 20);
constexpr int EDGE_X = 0, EDGE_O = 4, EDGE_SETUP = 8;
constexpr int XS_BUF = 128 * 1024;
constexpr int WS_XS = 262144;
constexpr int WS_KZ = WS_XS + 2 * XS_BUF;
constexpr int KZ_BUF = 16 * 4 * 1024;
constexpr int WS_CSP = WS_KZ + 2 * KZ_BUF;
constexpr int FRAG_BUF = 2 << 20;
constexpr int WS_KV = 1 << 20;
constexpr int KV_BUF = 16 * 16 * 4 * 1024;
constexpr int WS_XF = WS_KV + 2 * KV_BUF;
constexpr int WS_OF = WS_XF + 2 * FRAG_BUF;
constexpr int WS_WT = WS_OF + FRAG_BUF;
constexpr int WS_WOT = WS_WT + 2 * 16 * 98304;
constexpr int WS_END = FSTR == 8 ? WS_WOT + 2 * 16 * 65536 : (20 << 20);
constexpr int WS_STAMP = 32 << 20;

constexpr int LDS_X = 0, LDS_Y = 65536, LDS_Z = 131072;
constexpr int LDS_BYTES = 163840;
constexpr int AUX_SC1 = 16;
#define MAP_NJ 8
constexpr unsigned SPIN_CAP = 1u << 16;

DI unsigned pk2(float a, float b) { f32x2 v = {a, b}; return __builtin_bit_cast(unsigned, __builtin_convertvector(v, bf16x2)); }
DI float bflo(unsigned u) { return __uint_as_float(u << 16); }
DI float bfhi(unsigned u) { return __uint_as_float(u & 0xffff0000u); }
DI f32x4 mfma32(u32x4 a, u32x4 b, f32x4 c) {
  return __builtin_amdgcn_mfma_f32_16x16x32_bf16(__builtin_bit_cast(bf16x8, a), __builtin_bit_cast(bf16x8, b), c, 0, 0, 0);
}
DI f32x4 mfma16(u32x2 a, u32x2 b, f32x4 c) {
  return __builtin_amdgcn_mfma_f32_16x16x16bf16_1k(__builtin_bit_cast(s16x4, a), __builtin_bit_cast(s16x4, b), c, 0, 0, 0);
}
DI LAS unsigned char* lds_at(int off) { return (LAS unsigned char*)(uintptr_t)(unsigned)off; }
DI u32x2 tr_read(LAS unsigned char* p) { return __builtin_bit_cast(u32x2, __builtin_amdgcn_ds_read_tr16_b64_v4i16((LAS s16x4*)p)); }
DI u32x4 tr_frag(LAS unsigned char* p, int second) {
  u32x2 a = tr_read(p), b = tr_read(p + second);
  return u32x4{a.x, a.y, b.x, b.y};
}
DI void swap32(unsigned& a, unsigned& b) { auto r = __builtin_amdgcn_permlane32_swap(a, b, false, false); a = r[0]; b = r[1]; }
DI void swap16(unsigned& a, unsigned& b) { auto r = __builtin_amdgcn_permlane16_swap(a, b, false, false); a = r[0]; b = r[1]; }
DI void swap32f(float& a, float& b) { unsigned x = __float_as_uint(a), y = __float_as_uint(b); swap32(x, y); a = __uint_as_float(x); b = __uint_as_float(y); }
DI void swap16f(float& a, float& b) { unsigned x = __float_as_uint(a), y = __float_as_uint(b); swap16(x, y); a = __uint_as_float(x); b = __uint_as_float(y); }
DI float max3(float a, float b, float c) { return __builtin_fmaxf(__builtin_fmaxf(a, b), c); }
DI float sel4(f32x4 v, int i) { const float a = (i & 1) ? v.y : v.x, b = (i & 1) ? v.w : v.z; return (i & 2) ? b : a; }
DI float quad_sum(float s) {
  s += __builtin_bit_cast(float, __builtin_amdgcn_update_dpp(0, __builtin_bit_cast(int, s), 0xB1, 0xf, 0xf, true));
  s += __builtin_bit_cast(float, __builtin_amdgcn_update_dpp(0, __builtin_bit_cast(int, s), 0x4E, 0xf, 0xf, true));
  return s;
}
DI float sum16q(float s) {
  s += __builtin_bit_cast(float, __builtin_amdgcn_update_dpp(0, __builtin_bit_cast(int, s), 0x124, 0xf, 0xf, true));
  s += __builtin_bit_cast(float, __builtin_amdgcn_update_dpp(0, __builtin_bit_cast(int, s), 0x128, 0xf, 0xf, true));
  float a = s, b = s;
  swap16f(a, b);
  s = a + b; a = s; b = s;
  swap32f(a, b);
  return a + b;
}
DI f32x4 sum16q(u32x4 v) { return f32x4{sum16q(__uint_as_float(v.x)), sum16q(__uint_as_float(v.y)), sum16q(__uint_as_float(v.z)), sum16q(__uint_as_float(v.w))}; }
DI void xsum2(float& s1, float& s2) {
  swap32f(s1, s2);
  float t = s1 + s2, u = t;
  swap16f(t, u);
  float a = t + u, b = a;
  swap32f(a, b);
  s1 = a; s2 = b;
}
#define BAR() asm volatile("s_waitcnt lgkmcnt(0)\n\ts_barrier" ::: "memory")
#define DRAIN() asm volatile("s_waitcnt vmcnt(0)" ::: "memory")
#define SB() __builtin_amdgcn_sched_barrier(0)
#define OPAQUE_V(x) asm volatile("" : "+v"(x))
#define OPAQUE_S(x) asm volatile("" : "+s"(x))

DI void dma16(const void* sbase, unsigned voff, unsigned lds_addr) {
  unsigned keep;
  asm volatile("s_mov_b32 %0, m0\n\ts_mov_b32 m0, %3\n\ts_nop 2\n\tglobal_load_lds_dwordx4 %1, %2\n\ts_mov_b32 m0, %0"
               : "=&s"(keep) : "v"(voff), "s"(sbase), "s"(lds_addr) : "memory");
}

struct Ctx {
  __amdgpu_buffer_rsrc_t rs;
  unsigned tag0, tag1;
  int lane, wave, gi, gj;
};

DI u32x4 ld128(const Ctx& x, int voff, int soff) { return __builtin_amdgcn_raw_buffer_load_b128(x.rs, voff, soff, AUX_SC1); }
DI void st128(const Ctx& x, u32x4 v, int voff, int soff) { __builtin_amdgcn_raw_buffer_store_b128(v, x.rs, voff + soff, 0, AUX_SC1); }
DI void st64(const Ctx& x, u32x2 v, int voff, int soff) { __builtin_amdgcn_raw_buffer_store_b64(v, x.rs, voff, soff, AUX_SC1); }
DI void st32(const Ctx& x, unsigned v, int voff, int soff) { __builtin_amdgcn_raw_buffer_store_b32(v, x.rs, voff, soff, AUX_SC1); }

#define STAMP(n) do {} while (0)
#define STAMP_FLUSH() do {} while (0)

DI void signal(const Ctx& x, int lane, int word) {
  DRAIN();
  if (lane == 0) st64(x, u32x2{x.tag0, x.tag1}, 0, WS_FLAGS + word * FSTR);
}
#define POLL_PRE 14
#define POLL_GAP 0
template <int PRE = POLL_PRE>
DI void poll(const Ctx& x, int word) {
  if (PRE) __builtin_amdgcn_s_sleep(PRE);
  for (unsigned spins = 0; spins < SPIN_CAP; ++spins) {
    asm volatile("" ::: "memory");
    const u32x2 v = __builtin_amdgcn_raw_buffer_load_b64(x.rs, word * FSTR, WS_FLAGS, AUX_SC1);
    if (__all(v.x == x.tag0 && v.y == x.tag1)) break;
    if (POLL_GAP) __builtin_amdgcn_s_sleep(POLL_GAP);
  }
  __builtin_amdgcn_fence(__ATOMIC_ACQUIRE, "wavefront");
  asm volatile("" ::: "memory");
}

DI u32x4 ld_stats(const Ctx& x, int voff, int soff) {
  const u32x2 a = __builtin_amdgcn_raw_buffer_load_b64(x.rs, voff, soff, AUX_SC1), b = __builtin_amdgcn_raw_buffer_load_b64(x.rs, voff + 128, soff, AUX_SC1);
  return u32x4{a.x, a.y, b.x, b.y};
}
DI void finish_stats(u32x4 p, float& mean, float& rstd) {
  float s1 = __uint_as_float(p.x) + __uint_as_float(p.z), s2 = __uint_as_float(p.y) + __uint_as_float(p.w);
  xsum2(s1, s2);
  mean = s1 * (1.f / 512.f);
  float var = fmaxf(s2 * (1.f / 512.f) - mean * mean, 0.f);
  rstd = __builtin_amdgcn_rsqf(var + 1e-5f);
}
DI u32x2 ln_pack(f32x4 a, f32x4 cs, float mean, float rstd) {
  float v0 = fmaf(-mean, cs.x, a.x) * rstd, v1 = fmaf(-mean, cs.y, a.y) * rstd;
  float v2 = fmaf(-mean, cs.z, a.z) * rstd, v3 = fmaf(-mean, cs.w, a.w) * rstd;
  return u32x2{pk2(v0, v1), pk2(v2, v3)};
}

DI void feature_map(const u32x2 (&in)[2], const u32x2 (&wp)[2], unsigned (&o)[8]) {
  f32x4 p[4];
  const f32x4 z = {0.f, 0.f, 0.f, 0.f};
#pragma unroll
  for (int h = 0; h < 2; ++h) {
    p[2 * h] = mfma16(wp[h], in[h], z);
    p[2 * h + 1] = mfma16(u32x2{wp[h].x ^ 0x80008000u, wp[h].y ^ 0x80008000u}, in[h], z);
  }
  float v[16];
#pragma unroll
  for (int i = 0; i < 4; ++i)
#pragma unroll
    for (int r = 0; r < 4; ++r) v[4 * i + r] = p[i][r];
#pragma unroll
  for (int r = 0; r < 4; ++r) { swap32f(v[r], v[8 + r]); swap32f(v[4 + r], v[12 + r]); }
#pragma unroll
  for (int r = 0; r < 4; ++r) { swap16f(v[r], v[4 + r]); swap16f(v[8 + r], v[12 + r]); }
  const float m = max3(max3(max3(v[0], v[1], v[2]), max3(v[3], v[4], v[5]), max3(v[6], v[7], v[8])),
                       max3(max3(v[9], v[10], v[11]), max3(v[12], v[13], v[14]), v[15]), v[15]);
  const float LOG2E = 1.4426950408889634f;
  const float mm = -m * LOG2E;
#pragma unroll
  for (int i = 0; i < 16; ++i) v[i] = __builtin_amdgcn_exp2f(fmaf(v[i], LOG2E, mm));
  f32x2 s2 = {0.f, 0.f};
#pragma unroll
  for (int i = 0; i < 16; i += 4) s2 += (f32x2{v[i], v[i + 1]} + f32x2{v[i + 2], v[i + 3]});
  const float rs = __builtin_amdgcn_rcpf(s2.x + s2.y);
#pragma unroll
  for (int i = 0; i < 8; ++i) { const f32x2 t = f32x2{v[2 * i], v[2 * i + 1]} * rs; o[i] = pk2(t.x, t.y); }
}

#define XWIN 6
#define OWIN 8
#define START_SLEEP 4
#define DMA_SLEEP 1

#define WFRAG(s, ct) (*(LAS u32x4*)((ct) < 2 ? lds_at(wq) + (s) * 2048 + (ct) * 1024 : lds_at(wa) + (s) * 4096 + ((ct) - 2) * 1024))
template <bool IS_B>
DI void gemm_phase(const Ctx& x, int L, f32x2 cskeep, u32x4 (&xf)[16], f32x4 (&acc)[6], float& mean, float& rstd, u32x4& hsp, unsigned char* wsp) {
  constexpr int KVW = IS_B ? LDS_Y : LDS_X, SCR = IS_B ? LDS_X : LDS_Y;
  constexpr int LDS_HF = SCR + 4096, LDS_S = SCR + 4096 + 64;
  int lane = x.lane, w = x.wave, gi = x.gi, gj = x.gj;
  OPAQUE_V(lane); OPAQUE_S(w); OPAQUE_S(gi); OPAQUE_S(gj);
  const int c = lane & 15, g = lane >> 4;
  const int LDS_CS = SCR + w * 512;
  const int tau = 8 * gi + w;
  const bool has_halo = IS_B && gi > 0;
  constexpr int KIM = LDS_S, VIMB = KIM + 8192, HP = VIMB + 10240;
  const int wa = KVW + lane * 16, wq = LDS_Z + lane * 16;
  constexpr int C0 = IS_B ? 0 : 2;
  *(LAS float*)lds_at(LDS_CS + lane * 4) = cskeep.x;
  *(LAS float*)lds_at(LDS_CS + 256 + lane * 4) = cskeep.y;
  STAMP(4 + L * 12 + 0);
  int fo = ((EDGE_X + L) * 128 + tau) * 16 + (lane & 7);
  if (has_halo && lane >= 8 && lane < 24) fo = ((EDGE_X + L) * 128 + 8 * gi - 2 + ((lane >> 3) & 1)) * 16 + (w < 2 ? (lane & 7) : w);
  poll(x, fo);
  STAMP(4 + L * 12 + 1);
  constexpr int XW = XWIN, XB = IS_B ? 1 : 0;
  const int xbase = WS_XF + XB * FRAG_BUF + tau * 16384;
#define LDX(s) ld128(x, lane * 16 + ((s) & 3) * 1024, xbase + ((s) >> 2) * 4096)
  u32x4 hx[2], sp;
  const int hc = 8 + (c & 7);
#pragma unroll
  for (int s = 0; s < XW; ++s) {
    xf[s] = LDX(s);
    if (IS_B && s == 2) {
#pragma unroll
      for (int i = 0; i < 2; ++i)
        hx[i] = ld128(x, has_halo ? (8 * gi - 2 + (c >> 3)) * 16384 + (g * 16 + hc) * 16 : 0x7ffffff0, WS_XF + FRAG_BUF + w * 2048 + i * 1024);
    }
    if (s == 5) sp = ld_stats(x, g * 256 + c * 8, WS_XS + XB * XS_BUF + tau * 1024);
    SB();
  }
  if (IS_B && w < 2) hsp = ld_stats(x, has_halo ? (8 * gi - 2 + w) * 1024 + g * 256 + hc * 8 : 0x7fffff00, WS_XS + XS_BUF);
  u32x4 hf[4];
  f32x4 hacc[4];
#pragma unroll
  for (int ct = 0; ct < 6; ++ct) acc[ct] = f32x4{0.f, 0.f, 0.f, 0.f};
  u32x4 wf[2][6];
#pragma unroll
  for (int ct = C0; ct < 6; ++ct) wf[0][ct] = WFRAG(0, ct);
  SB();
#pragma unroll
  for (int s = 0; s < 16; ++s) {
    if (s + XW < 16) xf[s + XW] = LDX(s + XW);
    if (s + 1 < 16) {
#pragma unroll
      for (int ct = C0; ct < 6; ++ct) wf[(s + 1) & 1][ct] = WFRAG(s + 1, ct);
    }
#pragma unroll
    for (int ct = C0; ct < 6; ++ct) acc[ct] = mfma32(wf[s & 1][ct], xf[s], acc[ct]);
    if (IS_B) {
      if (s == 3 || s == 4) {
#pragma unroll
        for (int i = 0; i < 4; ++i) hacc[i] = mfma32(hf[i], hx[s - 3], s == 3 ? f32x4{0.f, 0.f, 0.f, 0.f} : hacc[i]);
      }
      if (s == 2 || s == 3) {
#pragma unroll
        for (int i = 0; i < 4; ++i) hf[i] = *(LAS u32x4*)(lds_at(wa + w * 8192) + (s - 2) * 4096 + i * 1024);
      }
      if (s == 5) {
#pragma unroll
        for (int i = 0; i < 4; ++i) *(LAS f32x4*)(lds_at(HP + w * 4096 + lane * 16) + i * 1024) = hacc[i];
      }
      if (s == 7) *(volatile LAS u32x2*)lds_at(LDS_HF + w * 8) = u32x2{x.tag0, x.tag1 + L};
    }
    if (s == 10) finish_stats(sp, mean, rstd);
    if (s + 1 < 16) {
#pragma unroll
      for (int i = C0; i < 6; ++i) {
        __builtin_amdgcn_sched_group_barrier(0x008, 1, 0);
        __builtin_amdgcn_sched_group_barrier(0x100, 1, 0);
        if (s == 10) __builtin_amdgcn_sched_group_barrier(0x002, 5, 0);
        if (IS_B && s == 5) __builtin_amdgcn_sched_group_barrier(0x200, 1, 0);
      }
      if (IS_B && s >= 2 && s <= 4) {
#pragma unroll
        for (int i = 0; i < 4; ++i) {
          __builtin_amdgcn_sched_group_barrier(0x008, 1, 0);
          __builtin_amdgcn_sched_group_barrier(0x100, 1, 0);
        }
      }
    }
    SB();
  }
#undef LDX
}

template <bool IS_B>
DI void mix_phase(const Ctx& x, int L, const u32x2 (&wp)[2], const u32x4 (&po)[8], const u32x4 (&xf)[16], f32x4 (&acc)[6], float mean, float rstd, u32x4 hsp,
                  unsigned char* wsp) {
  constexpr int KVW = IS_B ? LDS_Y : LDS_X, SCR = IS_B ? LDS_X : LDS_Y;
  constexpr int LDS_HF = SCR + 4096, LDS_S = SCR + 4096 + 64;
  int lane = x.lane, w = x.wave, gi = x.gi, gj = x.gj;
  OPAQUE_V(lane); OPAQUE_S(w); OPAQUE_S(gi); OPAQUE_S(gj);
  const int c = lane & 15, g = lane >> 4;
  const int LDS_CS = SCR + w * 512;
  const int tau = 8 * gi + w;
  const bool has_halo = IS_B && gi > 0;
  constexpr int KIM = LDS_S, VIMB = KIM + 8192, HP = VIMB + 10240;
  const int wa = KVW + lane * 16, wq = LDS_Z + lane * 16;
  constexpr int C0 = IS_B ? 0 : 2;
  const int q = c >> 2, p = c & 3;
  const u32x4 ones = {0x3f803f80u, 0x3f803f80u, 0x3f803f80u, 0x3f803f80u};
  f32x4 cs[6];
#pragma unroll
  for (int ct = C0; ct < 6; ++ct) cs[ct] = *(LAS f32x4*)(lds_at(LDS_CS + g * 16) + ct * 64);
  STAMP(4 + L * 12 + 2);
  const int obase = WS_OF + tau * 16384 + gj * 1024;
#define STAGE_WO() do { _Pragma("unroll") for (int n = 0; n < 8; ++n) *(LAS u32x4*)(lds_at(KVW + w * 8192 + lane * 16) + n * 1024) = po[n]; } while (0)
  if (!IS_B) {
    constexpr int PHI = LDS_S, VIM = PHI + 16384, KVOP = VIM + 8192, ZOP = KVOP + 2048;
    {
      const int row = w * 16 + c;
      u32x2 kn[2];
#pragma unroll
      for (int h = 0; h < 2; ++h) kn[h] = ln_pack(acc[2 + h], cs[2 + h], mean, rstd);
      unsigned ph[8];
      feature_map(kn, wp, ph);
#pragma unroll
      for (int h = 0; h < 2; ++h) *(LAS u32x2*)lds_at(VIM + h * 4096 + row * 32 + g * 8) = ln_pack(acc[4 + h], cs[4 + h], mean, rstd);
      *(LAS u32x4*)lds_at(PHI + g * 4096 + row * 32) = u32x4{ph[0], ph[1], ph[2], ph[3]};
      *(LAS u32x4*)lds_at(PHI + g * 4096 + row * 32 + 16) = u32x4{ph[4], ph[5], ph[6], ph[7]};
    }
    BAR();
    const int e = L >> 1;
    if (w < 4) {
      f32x4 kv0 = {0.f, 0.f, 0.f, 0.f}, kv1 = kv0, z0 = kv0, z1 = kv0;
      LAS unsigned char* pa = lds_at(PHI + w * 4096 + (4 * g + q) * 32 + p * 8);
      LAS unsigned char* pb = lds_at(VIM + (w >> 1) * 4096 + (4 * g + q) * 32 + p * 8);
#pragma unroll
      for (int u = 0; u < 4; u += 2) {
        const u32x4 a0 = tr_frag(pa + u * 1024, 512), b0 = tr_frag(pb + u * 1024, 512);
        const u32x4 a1 = tr_frag(pa + u * 1024 + 1024, 512), b1 = tr_frag(pb + u * 1024 + 1024, 512);
        kv0 = mfma32(a0, b0, kv0); kv1 = mfma32(a1, b1, kv1);
        z0 = mfma32(a0, ones, z0); z1 = mfma32(a1, ones, z1);
      }
      kv0 += kv1; z0 += z1;
      st128(x, __builtin_bit_cast(u32x4, kv0), lane * 16, WS_KV + e * KV_BUF + ((gj * 16 + gi) * 4 + w) * 1024);
      if (c == 0) st128(x, __builtin_bit_cast(u32x4, z0), g * 16, WS_KZ + e * KZ_BUF + (gj * 4 + w) * 1024 + gi * 64);
      STAGE_WO();
      signal(x, lane, (e * 64 + gj * 4 + w) * 16 + gi);
    } else {
      __builtin_amdgcn_s_setprio(1);
    }
    STAMP(4 + L * 12 + 3);
    u32x4 pq[2];
    {
#pragma unroll
      for (int s = 0; s < 16; s += 4) {
        u32x4 f[8];
#pragma unroll
        for (int i = 0; i < 8; ++i) f[i] = WFRAG(s + (i >> 1), i & 1);
#pragma unroll
        for (int i = 0; i < 8; ++i) acc[i & 1] = mfma32(f[i], xf[s + (i >> 1)], acc[i & 1]);
        SB();
      }
      u32x2 qn[2];
#pragma unroll
      for (int h = 0; h < 2; ++h) qn[h] = ln_pack(acc[h], *(LAS f32x4*)(lds_at(LDS_CS + g * 16) + h * 64), mean, rstd);
      unsigned ph[8];
      feature_map(qn, wp, ph);
#pragma unroll
      for (int r = 0; r < 2; ++r) { swap16(ph[r], ph[2 + r]); swap16(ph[4 + r], ph[6 + r]); }
#pragma unroll
      for (int r = 0; r < 2; ++r) { swap32(ph[r], ph[4 + r]); swap32(ph[2 + r], ph[6 + r]); }
      pq[0] = u32x4{ph[0], ph[1], ph[2], ph[3]};
      pq[1] = u32x4{ph[4], ph[5], ph[6], ph[7]};
      OPAQUE_V(pq[0]); OPAQUE_V(pq[1]);
    }
    STAMP(4 + L * 12 + 4);
    if (w >= 4) {
      const int wi = w - 4;
      poll<0>(x, (e * 64 + gj * 4 + wi) * 16 + c);
      STAMP(4 + L * 12 + 5);
      const int sbase = WS_KV + e * KV_BUF + (gj * 64 + wi) * 1024;
      const u32x4 zu = ld128(x, lane * 16, WS_KZ + e * KZ_BUF + (gj * 4 + wi) * 1024);
      u32x4 a[16];
#pragma unroll
      for (int i = 0; i < 16; ++i) a[i] = ld128(x, lane * 16, sbase + i * 4096);
      SB();
      STAGE_WO();
      const f32x4 zs = sum16q(zu);
      if (lane < 4) *(LAS u32x2*)lds_at(ZOP + (wi >> 1) * 64 + lane * 16 + (wi & 1) * 8) = u32x2{pk2(zs.x, zs.y), pk2(zs.z, zs.w)};
      f32x4 t[4];
#pragma unroll
      for (int i = 0; i < 4; ++i)
        t[i] = (__builtin_bit_cast(f32x4, a[4 * i]) + __builtin_bit_cast(f32x4, a[4 * i + 1])) + (__builtin_bit_cast(f32x4, a[4 * i + 2]) + __builtin_bit_cast(f32x4, a[4 * i + 3]));
      const f32x4 kv = (t[0] + t[1]) + (t[2] + t[3]);
      *(LAS u32x2*)lds_at(KVOP + (wi >> 1) * 1024 + lane * 16 + (wi & 1) * 8) = u32x2{pk2(kv.x, kv.y), pk2(kv.z, kv.w)};
      __builtin_amdgcn_s_setprio(0);
    }
    BAR();
    {
      const f32x4 z = {0.f, 0.f, 0.f, 0.f};
      unsigned o[4];
#pragma unroll
      for (int h = 0; h < 2; ++h) {
        const u32x4 kvop = *(LAS u32x4*)lds_at(KVOP + h * 1024 + lane * 16);
        const u32x4 zop = *(LAS u32x4*)lds_at(ZOP + h * 64 + g * 16);
        f32x4 num = mfma32(kvop, pq[h], z), den = mfma32(zop, pq[h], z);
        const float r = __builtin_amdgcn_rcpf(den.x + 1e-6f);
        o[2 * h] = pk2(num.x * r, num.y * r); o[2 * h + 1] = pk2(num.z * r, num.w * r);
      }
      st128(x, u32x4{o[0], o[1], o[2], o[3]}, lane * 16, obase);
    }
  } else {
    constexpr int VIM = VIMB;
    u32x2 qn[2], kn[2], kp[2];
#pragma unroll
    for (int h = 0; h < 2; ++h) {
      kn[h] = ln_pack(acc[2 + h], cs[2 + h], mean, rstd);
      *(LAS u32x2*)lds_at(KIM + (w * 2 + h) * 512 + lane * 8) = kn[h];
      *(LAS u32x2*)lds_at(VIM + ((2 + w) * 2 + h) * 512 + c * 32 + g * 8) = ln_pack(acc[4 + h], cs[4 + h], mean, rstd);
    }
    if (w < 2) {
      for (unsigned spins = 0; spins < SPIN_CAP; ++spins) {
        const u32x2 v = *(volatile LAS u32x2*)lds_at(LDS_HF + (lane & 7) * 8);
        if (__all(v.x == x.tag0 && v.y == x.tag1 + L)) break;
      }
      float hm, hr;
      finish_stats(hsp, hm, hr);
      const int src = HP + (c < 8 ? 2048 : 0) + (g * 16 + w * 8 + (c & 7)) * 16;
#pragma unroll
      for (int h = 0; h < 2; ++h) {
        f32x4 a[8];
#pragma unroll
        for (int i = 0; i < 8; ++i) a[i] = *(LAS f32x4*)(lds_at(src) + i * 4096 + h * 1024);
        const f32x4 sum = ((a[0] + a[1]) + (a[2] + a[3])) + ((a[4] + a[5]) + (a[6] + a[7]));
        const f32x4 csw = c < 8 ? cs[4 + h] : cs[2 + h];
        u32x2 v = ln_pack(sum, csw, hm, hr);
        if (!has_halo) v = u32x2{0u, 0u};
        kp[h] = c < 8 ? u32x2{0u, 0u} : v;
        *(LAS u32x2*)lds_at(VIM + (w * 2 + h) * 512 + (c ^ 8) * 32 + g * 8) = c < 8 ? v : u32x2{0u, 0u};
      }
    }
#pragma unroll
    for (int h = 0; h < 2; ++h) qn[h] = ln_pack(acc[h], cs[h], mean, rstd);
    const float NEG = -1e30f;
    f32x4 bo, bp;
    const bool noprev = (w < 2) && !has_halo;
#pragma unroll
    for (int r = 0; r < 4; ++r) {
      const int key = 4 * g + r;
      bo[r] = (c - key >= 0 && c - key <= 7) ? 0.f : NEG;
      bp[r] = (key >= c + 9 && !noprev) ? 0.f : NEG;
    }
    f32x4 so[2];
#pragma unroll
    for (int h = 0; h < 2; ++h) so[h] = mfma16(kn[h], qn[h], bo);
    BAR();
    {
      const f32x4 z = {0.f, 0.f, 0.f, 0.f};
      const float SC = 0.25f * 1.4426950408889634f;
      f32x4 sp_[2];
      u32x2 vo[2], vp[2];
      float m[2];
#pragma unroll
      for (int h = 0; h < 2; ++h) {
        if (w >= 2) kp[h] = *(LAS u32x2*)lds_at(KIM + ((w - 2) * 2 + h) * 512 + lane * 8);
        vp[h] = tr_read(lds_at(VIM + (w * 2 + h) * 512 + (4 * g + q) * 32 + p * 8));
        vo[h] = tr_read(lds_at(VIM + ((2 + w) * 2 + h) * 512 + (4 * g + q) * 32 + p * 8));
      }
#pragma unroll
      for (int h = 0; h < 2; ++h) {
        sp_[h] = mfma16(kp[h], qn[h], bp);
        m[h] = max3(max3(so[h].x, so[h].y, so[h].z), max3(so[h].w, sp_[h].x, sp_[h].y), max3(sp_[h].z, sp_[h].w, sp_[h].w));
      }
      swap32f(m[0], m[1]);
      float t = max3(m[0], m[1], m[1]), u = t;
      swap16f(t, u);
      float a = max3(t, u, u), b = a;
      swap32f(a, b);
      m[0] = a; m[1] = b;
      unsigned o[4];
#pragma unroll
      for (int h = 0; h < 2; ++h) {
        const float mm = -m[h] * SC;
        float e[8];
#pragma unroll
        for (int r = 0; r < 4; ++r) { e[r] = __builtin_amdgcn_exp2f(fmaf(so[h][r], SC, mm)); e[4 + r] = __builtin_amdgcn_exp2f(fmaf(sp_[h][r], SC, mm)); }
        const u32x4 P = {pk2(e[0], e[1]), pk2(e[2], e[3]), pk2(e[4], e[5]), pk2(e[6], e[7])};
        f32x4 num = mfma32(u32x4{vo[h].x, vo[h].y, vp[h].x, vp[h].y}, P, z), den = mfma32(ones, P, z);
        const float r = __builtin_amdgcn_rcpf(den.x);
        o[2 * h] = pk2(num.x * r, num.y * r); o[2 * h + 1] = pk2(num.z * r, num.w * r);
      }
      st128(x, u32x4{o[0], o[1], o[2], o[3]}, lane * 16, obase);
    }
    SB();
    STAGE_WO();
  }
  STAMP(4 + L * 12 + 6);
  signal(x, lane, ((EDGE_O + L) * 128 + tau) * 16 + gj);
  BAR();
#undef WFRAG
#undef STAGE_WO
}

template <bool IS_B>
DI void oproj_phase(const Ctx& x, int L, f32x4 (&res)[4], u32x4 (&poB)[8], f32x2& csB, u16* out, unsigned char* wsp) {
  constexpr int KVW = IS_B ? LDS_Y : LDS_X, SCR = IS_B ? LDS_X : LDS_Y;
  int lane = x.lane, w = x.wave, gi = x.gi, gj = x.gj;
  OPAQUE_V(lane); OPAQUE_S(w); OPAQUE_S(gi); OPAQUE_S(gj);
  const int c = lane & 15, g = lane >> 4;
  const int tau = 8 * gi + 4 * (gj >> 3) + w;
  STAMP(4 + L * 12 + 7);
  f32x4 cb[2] = {f32x4{0.f, 0.f, 0.f, 0.f}, f32x4{0.f, 0.f, 0.f, 0.f}};
  if (w < 4) {
    const int wb = KVW + lane * 16;
    constexpr int PD = 3, OW = OWIN;
    u32x4 wf[PD][4];
#pragma unroll
    for (int s = 0; s < PD; ++s)
#pragma unroll
      for (int ct = 0; ct < 4; ++ct) wf[s][ct] = *(LAS u32x4*)(lds_at(wb) + s * 4096 + ct * 1024);
    poll(x, ((EDGE_O + L) * 128 + tau) * 16 + c);
    STAMP(4 + L * 12 + 8);
    const int obase = WS_OF + tau * 16384;
    u32x4 of[16];
#pragma unroll
    for (int s = 0; s < OW; ++s) { of[s] = ld128(x, lane * 16 + (s & 3) * 1024, obase + (s >> 2) * 4096); SB(); }
#pragma unroll
    for (int s = 0; s < 16; ++s) {
      if (s + OW < 16) of[s + OW] = ld128(x, lane * 16 + ((s + OW) & 3) * 1024, obase + ((s + OW) >> 2) * 4096);
#pragma unroll
      for (int ct = 0; ct < 4; ++ct) {
        res[ct] = mfma32(wf[s % PD][ct], of[s], res[ct]);
        if (s + PD < 16) wf[s % PD][ct] = *(LAS u32x4*)(lds_at(wb) + (s + PD) * 4096 + ct * 1024);
      }
      SB();
    }
    STAMP(4 + L * 12 + 9);
    const int nb = gj & 7;
    u32x4 f[2];
#pragma unroll
    for (int a = 0; a < 2; ++a)
      f[a] = u32x4{pk2(res[2 * a].x, res[2 * a].y), pk2(res[2 * a].z, res[2 * a].w), pk2(res[2 * a + 1].x, res[2 * a + 1].y), pk2(res[2 * a + 1].z, res[2 * a + 1].w)};
    if (L < 3) {
#pragma unroll
      for (int a = 0; a < 2; ++a) st128(x, f[a], lane * 16 + a * 1024, WS_XF + (IS_B ? 0 : FRAG_BUF) + (tau * 16 + nb * 2) * 1024);
      const f32x4 s = (res[0] + res[1]) + (res[2] + res[3]);
      const f32x4 qq = (res[0] * res[0] + res[1] * res[1]) + (res[2] * res[2] + res[3] * res[3]);
      float s1 = (s.x + s.y) + (s.z + s.w), s2 = (qq.x + qq.y) + (qq.z + qq.w);
      xsum2(s1, s2);
      if (lane < 16) st64(x, u32x2{__float_as_uint(s1), __float_as_uint(s2)}, lane * 8, WS_XS + (IS_B ? 0 : XS_BUF) + tau * 1024 + nb * 128);
      signal(x, lane, ((EDGE_X + L + 1) * 128 + tau) * 16 + nb);
    } else {
      const int tok = 32 * (tau >> 1) + 2 * c + (tau & 1);
#pragma unroll
      for (int a = 0; a < 2; ++a) *(GAS u32x4*)(out + tok * 512 + nb * 64 + a * 32 + 8 * g) = f[a];
    }
    STAMP(4 + L * 12 + 10);
  } else if (L < 3) {
    if (!IS_B && L == 0) {
      poll<0>(x, (EDGE_SETUP * 128 + 64 + gj * 4) * 16 + (lane < 48 ? lane : 0));
#pragma unroll
      for (int n = 0; n < 2; ++n) cb[n] = sum16q(ld128(x, lane * 16, WS_CSP + (16 + gj) * 6144 + ((w < 7 ? w - 4 : 0) * 2 + n) * 1024));
    }
    const int src = WS_WT + ((IS_B ? 0 : 16) + gj) * 98304;
    const int wi = w - 4;
    u32x4 f[24];
#define FSRC(n) ((n) < 16 ? src + wi * 16384 + ((n) >> 2) * 4096 : src + 65536 + wi * 8192 + (((n) - 16) >> 2) * 4096)
#define FDST(n) ((n) < 16 ? lds_at(SCR + wi * 16384 + lane * 16) + (n) * 1024 : lds_at(LDS_Z + wi * 8192 + lane * 16) + ((n) - 16) * 1024)
#pragma unroll
    for (int n = 0; n < 24; ++n) {
      f[n] = ld128(x, lane * 16 + (n & 3) * 1024, FSRC(n));
      __builtin_amdgcn_s_sleep(DMA_SLEEP);
      if (n >= 6) *(LAS u32x4*)FDST(n - 6) = f[n - 6];
      SB();
    }
#pragma unroll
    for (int n = 18; n < 24; ++n) *(LAS u32x4*)FDST(n) = f[n];
#undef FSRC
#undef FDST
    STAMP(4 + L * 12 + 10);
  }
  if (!IS_B && L == 0) {
    if (w < 4) poll<0>(x, (EDGE_SETUP * 128 + 64 + gj * 4) * 16 + (lane < 48 ? lane : 0));
#pragma unroll
    for (int n = 0; n < 8; ++n) poB[n] = ld128(x, lane * 16 + (n & 3) * 1024, WS_WOT + (16 + gj) * 65536 + w * 8192 + (n >> 2) * 4096);
    BAR();
    if (w >= 4 && w < 7 && lane < 4) {
#pragma unroll
      for (int n = 0; n < 2; ++n) *(LAS f32x4*)lds_at(KVW + 61440 + ((w - 4) * 2 + n) * 64 + lane * 16) = cb[n];
    }
    BAR();
    csB = f32x2{*(LAS float*)lds_at(KVW + 61440 + lane * 4), *(LAS float*)lds_at(KVW + 61440 + 256 + (lane & 31) * 4)};
  } else
  if (L < 3) BAR();
}

__global__ void __launch_bounds__(512, 2)
fused_fwd(const u16* __restrict__ x0, const u16* __restrict__ wA, const u16* __restrict__ wphi, const u16* __restrict__ woA,
          const u16* __restrict__ wB, const u16* __restrict__ woB, u16* __restrict__ out, unsigned char* __restrict__ wsp) {
  extern __shared__ __attribute__((aligned(16))) unsigned char smem[];
  Ctx x;
  x.lane = threadIdx.x & 63;
  x.wave = __builtin_amdgcn_readfirstlane(threadIdx.x >> 6);
  {
    constexpr int NJ = MAP_NJ, NI = 32 / NJ, XJ = 16 / NJ;
    const int xc = blockIdx.x & 7, r = blockIdx.x >> 3;
    x.gi = (xc / XJ) * NI + r / NJ;
    x.gj = (xc % XJ) * NJ + r % NJ;
  }
  x.rs = __builtin_amdgcn_make_buffer_rsrc((void*)wsp, 0, WS_END, 0x00020000);
  STAMP(0);
  const int lane = x.lane, w = x.wave, c = lane & 15, g = lane >> 4, q = c >> 2, p = c & 3, gi = x.gi, gj = x.gj;
  const u32x4 ones = {0x3f803f80u, 0x3f803f80u, 0x3f803f80u, 0x3f803f80u};
  {
    const unsigned long long t = *(const __attribute__((address_space(4))) unsigned long long*)(uintptr_t)(wsp + WS_EPOCH) + 1ull;
    x.tag0 = (unsigned)t; x.tag1 = (unsigned)(t >> 32);
  }
  const int tau0 = 8 * gi + w;
  const int xstg = (w < 4 ? LDS_Y : LDS_X) + (w & 3) * 16384;
  f32x4 res[4];
  auto unit_dma = [&](int u, int slot) {
    const bool isw = u < 6;
    const char* src = isw ? (const char*)(u < 3 ? wA : wB) + (size_t)gi * 98304 + (u % 3) * 1024 + gj * 64
                          : (const char*)(u < 8 ? woA : woB) + (size_t)gi * 32768 + (gj & 7) * 128 + (u & 1) * 64;
    const int ld = isw ? 3072 : 1024;
    const unsigned vo = (lane >> 2) * ld + (lane & 3) * 16;
    dma16(src, vo, slot);
    dma16(src + 16 * ld, vo, slot + 1024);
  };
  auto unit_read = [&](int u, int slot, u32x4 (&f)[2]) {
    const bool isw = u < 6;
    const int ad = slot + (isw ? (8 * g + q) * 64 + p * 8 : (4 * g + q) * 64 + p * 16);
    const int sec = isw ? 256 : 1024, astr = isw ? 32 : 8;
#pragma unroll
    for (int a = 0; a < 2; ++a) {
      const u32x2 lo = tr_read(lds_at(ad + a * astr)), hi = tr_read(lds_at(ad + a * astr + sec));
      f[a] = u32x4{lo.x, lo.y, hi.x, hi.y};
    }
  };
  auto unit_store = [&](int u, const u32x4 (&f)[2]) {
    const bool isw = u < 6;
    const int s3 = u % 3;
    const int dst = isw ? WS_WT + ((u / 3) * 16 + gj) * 98304 + (s3 == 0 ? 65536 + gi * 2048 : gi * 4096 + (s3 - 1) * 2048)
                        : WS_WOT + (((u - 6) >> 1) * 16 + gj) * 65536 + gi * 4096 + (u & 1) * 2048;
#pragma unroll
    for (int a = 0; a < 2; ++a) st128(x, f[a], lane * 16 + a * 1024, dst);
    if (isw) {
      const f32x4 s0 = mfma32(f[0], ones, f32x4{0.f, 0.f, 0.f, 0.f}), s1 = mfma32(f[1], ones, f32x4{0.f, 0.f, 0.f, 0.f});
      if (c < 2) st128(x, __builtin_bit_cast(u32x4, c ? s1 : s0), g * 16 + c * 1024, WS_CSP + (((u / 3) * 16 + gj) * 6 + s3 * 2) * 1024 + gi * 64);
    }
  };
  const int zs = LDS_Z + (w & 3) * 8192;
  if (w < 4) {
    const int u0 = w < 3 ? w : 6;
    unit_dma(u0, zs);
    if (w == 3) unit_dma(7, zs + 2048);
    DRAIN();
    STAMP(52);
    u32x4 f[2];
    unit_read(u0, zs, f);
    unit_store(u0, f);
    if (w == 3) { unit_read(7, zs + 2048, f); unit_store(7, f); }
    signal(x, lane, (EDGE_SETUP * 128 + gj * 4 + w) * 16 + gi);
  } else {
    __builtin_amdgcn_s_sleep(START_SLEEP);
  }
  STAMP(53);
  {
    const char* xrow = (const char*)x0 + (32 * (tau0 >> 1) + (tau0 & 1)) * 1024;
#pragma unroll
    for (int n = 0; n < 16; ++n) dma16(xrow + n * 2048, (lane ^ (n << 2)) << 4, xstg + n * 1024);
  }
  if (w < 4) {
    if (w < 3) unit_dma(3 + w, zs + 2048);
    if (w < 2) unit_dma(8 + w, zs + 4096);
    const int tau = 8 * gi + 4 * (gj >> 3) + w;
    const unsigned rvo = (32 * (tau >> 1) + 2 * (lane >> 3) + (tau & 1)) * 1024 + ((lane & 7) << 4);
    dma16((const char*)x0 + (gj & 7) * 128, rvo, zs + 6144);
    dma16((const char*)x0 + (gj & 7) * 128 + 16384, rvo, zs + 7168);
  }
  STAMP(56);
  u32x2 wp[2];
#pragma unroll
  for (int h = 0; h < 2; ++h) {
    const GAS u16* pw = (const GAS u16*)wphi + ((2 * gj + h) * 16 + 4 * g) * 16 + c;
    const unsigned a0 = pw[0], a1 = pw[16], a2 = pw[32], a3 = pw[48];
    wp[h] = u32x2{a0 | (a1 << 16), a2 | (a3 << 16)};
  }
  DRAIN();
  OPAQUE_S(x.tag0); OPAQUE_S(x.tag1);
  STAMP(1);
  u32x4 xf[16], fd0[2], fd1[2];
#pragma unroll
  for (int s = 0; s < 16; ++s) xf[s] = *(LAS u32x4*)lds_at(xstg + c * 1024 + ((s ^ c) << 6) + g * 16);
  unit_read(3, zs + 2048, fd0);
  unit_read(8, zs + 4096, fd1);
#pragma unroll
  for (int a = 0; a < 2; ++a) {
    const u32x4 v = *(LAS u32x4*)lds_at(zs + 6144 + c * 128 + ((a * 4 + g) << 4));
    res[2 * a] = f32x4{bflo(v.x), bfhi(v.x), bflo(v.y), bfhi(v.y)};
    res[2 * a + 1] = f32x4{bflo(v.z), bfhi(v.z), bflo(v.w), bfhi(v.w)};
  }
  asm volatile("s_waitcnt lgkmcnt(0)" : "+v"(xf[0]), "+v"(xf[1]), "+v"(xf[2]), "+v"(xf[3]), "+v"(xf[4]), "+v"(xf[5]), "+v"(xf[6]), "+v"(xf[7]),
               "+v"(xf[8]), "+v"(xf[9]), "+v"(xf[10]), "+v"(xf[11]), "+v"(xf[12]), "+v"(xf[13]), "+v"(xf[14]), "+v"(xf[15]),
               "+v"(fd0[0]), "+v"(fd0[1]), "+v"(fd1[0]), "+v"(fd1[1]), "+v"(res[0]), "+v"(res[1]), "+v"(res[2]), "+v"(res[3]) :: "memory");
  STAMP(54);
  poll<0>(x, (EDGE_SETUP * 128 + gj * 4) * 16 + lane);
  STAMP(55);
  float mean, rstd;
  auto row_stats = [&]() {
    f32x4 accs = {0.f, 0.f, 0.f, 0.f}, accq = accs;
#pragma unroll
    for (int n = 0; n < 16; ++n) { accs = mfma32(ones, xf[n], accs); accq = mfma32(xf[n], xf[n], accq); }
    const float dq = sel4(accq, p);
    finish_stats(u32x4{__float_as_uint(accs.x * 0.25f), g == q ? __float_as_uint(dq) : 0u, 0u, 0u}, mean, rstd);
    OPAQUE_V(mean); OPAQUE_V(rstd);
  };
  f32x2 csA;
  if (w >= 4) {
    u32x4 f[16];
#pragma unroll
    for (int n = 0; n < 16; ++n) { f[n] = ld128(x, lane * 16 + (n & 3) * 1024, WS_WT + gj * 98304 + (w - 4) * 16384 + (n >> 2) * 4096); SB(); }
    row_stats();
#pragma unroll
    for (int n = 0; n < 16; ++n) *(LAS u32x4*)(lds_at(xstg + lane * 16) + n * 1024) = f[n];
  } else {
    u32x4 cp[2], f[8];
#pragma unroll
    for (int n = 0; n < 2; ++n) cp[n] = ld128(x, lane * 16, WS_CSP + gj * 6144 + ((w < 3 ? w : 0) * 2 + n) * 1024);
#pragma unroll
    for (int n = 0; n < 8; ++n) { f[n] = ld128(x, lane * 16 + (n & 3) * 1024, WS_WT + gj * 98304 + 65536 + w * 8192 + (n >> 2) * 4096); SB(); }
    row_stats();
#pragma unroll
    for (int n = 0; n < 2; ++n) {
      const f32x4 s = sum16q(cp[n]);
      if (lane < 4) *(LAS f32x4*)lds_at(xstg + 8192 + n * 64 + lane * 16) = s;
    }
#pragma unroll
    for (int n = 0; n < 8; ++n) *(LAS u32x4*)(lds_at(LDS_Z + w * 8192 + lane * 16) + n * 1024) = f[n];
  }
  STAMP(2);
  BAR();
  csA = f32x2{*(LAS float*)lds_at(LDS_Y + 8192 + (lane >> 5) * 16384 + (lane & 31) * 4), *(LAS float*)lds_at(LDS_Y + 8192 + 32768 + (lane & 31) * 4)};
  if (w < 3) unit_store(3 + w, fd0);
  if (w < 2) unit_store(8 + w, fd1);
  u32x4 poA[8], poB[8];
  f32x4 acc[6];
#pragma unroll
  for (int ct = 0; ct < 6; ++ct) acc[ct] = f32x4{0.f, 0.f, 0.f, 0.f};
  {
    u32x4 wf[2][4];
#pragma unroll
    for (int ct = 0; ct < 4; ++ct) wf[0][ct] = *(LAS u32x4*)(lds_at(LDS_X + lane * 16) + ct * 1024);
    SB();
#pragma unroll
    for (int s = 0; s < 16; ++s) {
      if (!(s & 1)) poA[s >> 1] = ld128(x, lane * 16 + ((s >> 1) & 3) * 1024, WS_WOT + gj * 65536 + w * 8192 + (s >> 3) * 4096);
      if (s + 1 < 16) {
#pragma unroll
        for (int ct = 0; ct < 4; ++ct) wf[(s + 1) & 1][ct] = *(LAS u32x4*)(lds_at(LDS_X + lane * 16) + (s + 1) * 4096 + ct * 1024);
      }
#pragma unroll
      for (int ct = 0; ct < 4; ++ct) acc[2 + ct] = mfma32(wf[s & 1][ct], xf[s], acc[2 + ct]);
      if (s + 1 < 16) {
#pragma unroll
        for (int i = 0; i < 4; ++i) {
          __builtin_amdgcn_sched_group_barrier(0x008, 1, 0);
          __builtin_amdgcn_sched_group_barrier(0x100, 1, 0);
        }
      }
      SB();
    }
  }
  if (w < 3) signal(x, lane, (EDGE_SETUP * 128 + 64 + gj * 4 + w) * 16 + gi);
  BAR();
  f32x2 csB = {0.f, 0.f};
  *(LAS float*)lds_at(LDS_Y + w * 512 + lane * 4) = csA.x;
  *(LAS float*)lds_at(LDS_Y + w * 512 + 256 + lane * 4) = csA.y;
#pragma unroll
  for (int n = 0; n < 8; ++n) poB[n] = u32x4{0u, 0u, 0u, 0u};
  u32x4 hsp = {0u, 0u, 0u, 0u};
#pragma unroll 1
  for (int L = 0;; L = 2) {
    mix_phase<false>(x, L, wp, poA, xf, acc, mean, rstd, hsp, wsp);
    oproj_phase<false>(x, L, res, poB, csB, out, wsp);
    if (L == 0 && blockIdx.x == 0 && threadIdx.x == 0) st64(x, u32x2{x.tag0, x.tag1}, 0, WS_EPOCH);
    gemm_phase<true>(x, L + 1, csB, xf, acc, mean, rstd, hsp, wsp);
    mix_phase<true>(x, L + 1, wp, poB, xf, acc, mean, rstd, hsp, wsp);
    oproj_phase<true>(x, L + 1, res, poB, csB, out, wsp);
    if (L) break;
    gemm_phase<false>(x, 2, csA, xf, acc, mean, rstd, hsp, wsp);
  }
  STAMP(3);
  STAMP_FLUSH();
}

extern "C" void kernel_launch(void* const* d_in, const int* in_sizes, int n_in, void* d_out, int out_size, void* d_ws, size_t ws_size,
                              hipStream_t stream) {
  (void)in_sizes; (void)n_in; (void)out_size; (void)ws_size;
  (void)hipFuncSetAttribute((const void*)fused_fwd, hipFuncAttributeMaxDynamicSharedMemorySize, LDS_BYTES);
  fused_fwd<<<256, 512, LDS_BYTES, stream>>>((const u16*)d_in[0], (const u16*)d_in[1], (const u16*)d_in[2], (const u16*)d_in[3],
                                             (const u16*)d_in[5], (const u16*)d_in[6], (u16*)d_out, (unsigned char*)d_ws);
}
```

```cpp
#include <hip/hip_runtime.h>
#include <stdint.h>

typedef unsigned short u16;
typedef __bf16 bf16x8 __attribute__((ext_vector_type(8)));
typedef __bf16 bf16x2 __attribute__((ext_vector_type(2)));
typedef short s16x4 __attribute__((ext_vector_type(4)));
typedef float f32x4 __attribute__((ext_vector_type(4)));
typedef float f32x2 __attribute__((ext_vector_type(2)));
typedef unsigned u32x2 __attribute__((ext_vector_type(2)));
typedef unsigned u32x4 __attribute__((ext_vector_type(4)));
#define LAS __attribute__((address_space(3)))
#define GAS __attribute__((address_space(1)))
#define DI __device__ __forceinline__

constexpr int WS_EPOCH = 0;
#define FSTR 8
constexpr int WS_FLAGS = FSTR == 8 ? 4096 : (24 << 20);
constexpr int EDGE_X = 0, EDGE_O = 4, EDGE_SETUP = 8;
constexpr int XS_BUF = 128 * 1024;
constexpr int WS_XS = 262144 - XS_BUF;
constexpr int WS_KZ = WS_XS + 4 * XS_BUF;
constexpr int KZ_BUF = 16 * 4 * 1024;
constexpr int WS_CSP = WS_KZ + 2 * KZ_BUF;
constexpr int FRAG_BUF = 2 << 20;
constexpr int WS_KV = 1 << 20;
constexpr int KV_BUF = 16 * 16 * 4 * 1024;
constexpr int WS_XF = WS_KV + 2 * KV_BUF - FRAG_BUF;
constexpr int WS_OF = WS_XF + 4 * FRAG_BUF;
constexpr int WS_WT = WS_OF + 4 * FRAG_BUF;
constexpr int WS_WOT = WS_WT + 2 * 16 * 98304;
constexpr int WS_END = FSTR == 8 ? WS_WOT + 2 * 16 * 65536 : (28 << 20);
constexpr int WS_STAMP = 32 << 20;

constexpr int LDS_X = 0, LDS_Y = 65536, LDS_Z = 131072;
constexpr int LDS_BYTES = 163840;
constexpr int AUX_SC1 = 16;
#define MAP_NJ 8
constexpr unsigned SPIN_CAP = 1u << 16;

DI unsigned pk2(float a, float b) { f32x2 v = {a, b}; return __builtin_bit_cast(unsigned, __builtin_convertvector(v, bf16x2)); }
DI float bflo(unsigned u) { return __uint_as_float(u << 16); }
DI float bfhi(unsigned u) { return __uint_as_float(u & 0xffff0000u); }
DI f32x4 mfma32(u32x4 a, u32x4 b, f32x4 c) {
  return __builtin_amdgcn_mfma_f32_16x16x32_bf16(__builtin_bit_cast(bf16x8, a), __builtin_bit_cast(bf16x8, b), c, 0, 0, 0);
}
DI f32x4 mfma16(u32x2 a, u32x2 b, f32x4 c) {
  return __builtin_amdgcn_mfma_f32_16x16x16bf16_1k(__builtin_bit_cast(s16x4, a), __builtin_bit_cast(s16x4, b), c, 0, 0, 0);
}
DI LAS unsigned char* lds_at(int off) { return (LAS unsigned char*)(uintptr_t)(unsigned)off; }
DI u32x2 tr_read(LAS unsigned char* p) { return __builtin_bit_cast(u32x2, __builtin_amdgcn_ds_read_tr16_b64_v4i16((LAS s16x4*)p)); }
DI u32x4 tr_frag(LAS unsigned char* p, int second) {
  u32x2 a = tr_read(p), b = tr_read(p + second);
  return u32x4{a.x, a.y, b.x, b.y};
}
DI void swap32(unsigned& a, unsigned& b) { auto r = __builtin_amdgcn_permlane32_swap(a, b, false, false); a = r[0]; b = r[1]; }
DI void swap16(unsigned& a, unsigned& b) { auto r = __builtin_amdgcn_permlane16_swap(a, b, false, false); a = r[0]; b = r[1]; }
DI void swap32f(float& a, float& b) { unsigned x = __float_as_uint(a), y = __float_as_uint(b); swap32(x, y); a = __uint_as_float(x); b = __uint_as_float(y); }
DI void swap16f(float& a, float& b) { unsigned x = __float_as_uint(a), y = __float_as_uint(b); swap16(x, y); a = __uint_as_float(x); b = __uint_as_float(y); }
DI float max3(float a, float b, float c) { return __builtin_fmaxf(__builtin_fmaxf(a, b), c); }
DI float sel4(f32x4 v, int i) { const float a = (i & 1) ? v.y : v.x, b = (i & 1) ? v.w : v.z; return (i & 2) ? b : a; }
DI float quad_sum(float s) {
  s += __builtin_bit_cast(float, __builtin_amdgcn_update_dpp(0, __builtin_bit_cast(int, s), 0xB1, 0xf, 0xf, true));
  s += __builtin_bit_cast(float, __builtin_amdgcn_update_dpp(0, __builtin_bit_cast(int, s), 0x4E, 0xf, 0xf, true));
  return s;
}
DI float sum16q(float s) {
  s += __builtin_bit_cast(float, __builtin_amdgcn_update_dpp(0, __builtin_bit_cast(int, s), 0x124, 0xf, 0xf, true));
  s += __builtin_bit_cast(float, __builtin_amdgcn_update_dpp(0, __builtin_bit_cast(int, s), 0x128, 0xf, 0xf, true));
  float a = s, b = s;
  swap16f(a, b);
  s = a + b; a = s; b = s;
  swap32f(a, b);
  return a + b;
}
DI f32x4 sum16q(u32x4 v) { return f32x4{sum16q(__uint_as_float(v.x)), sum16q(__uint_as_float(v.y)), sum16q(__uint_as_float(v.z)), sum16q(__uint_as_float(v.w))}; }
DI void xsum2(float& s1, float& s2) {
  swap32f(s1, s2);
  float t = s1 + s2, u = t;
  swap16f(t, u);
  float a = t + u, b = a;
  swap32f(a, b);
  s1 = a; s2 = b;
}
#define BAR() asm volatile("s_waitcnt lgkmcnt(0)\n\ts_barrier" ::: "memory")
#define DRAIN() asm volatile("s_waitcnt vmcnt(0)" ::: "memory")
#define SB() __builtin_amdgcn_sched_barrier(0)
#define OPAQUE_V(x) asm volatile("" : "+v"(x))
#define OPAQUE_S(x) asm volatile("" : "+s"(x))

DI void dma16(const void* sbase, unsigned voff, unsigned lds_addr) {
  unsigned keep;
  asm volatile("s_mov_b32 %0, m0\n\ts_mov_b32 m0, %3\n\ts_nop 2\n\tglobal_load_lds_dwordx4 %1, %2\n\ts_mov_b32 m0, %0"
               : "=&s"(keep) : "v"(voff), "s"(sbase), "s"(lds_addr) : "memory");
}

struct Ctx {
  __amdgpu_buffer_rsrc_t rs;
  unsigned tag0, tag1;
  int lane, wave, gi, gj;
};

DI u32x4 ld128(const Ctx& x, int voff, int soff) { return __builtin_amdgcn_raw_buffer_load_b128(x.rs, voff, soff, AUX_SC1); }
DI void st128(const Ctx& x, u32x4 v, int voff, int soff) { __builtin_amdgcn_raw_buffer_store_b128(v, x.rs, voff + soff, 0, AUX_SC1); }
DI void st64(const Ctx& x, u32x2 v, int voff, int soff) { __builtin_amdgcn_raw_buffer_store_b64(v, x.rs, voff, soff, AUX_SC1); }
DI void st32(const Ctx& x, unsigned v, int voff, int soff) { __builtin_amdgcn_raw_buffer_store_b32(v, x.rs, voff, soff, AUX_SC1); }

#define STAMP(n) do {} while (0)
#define STAMP_FLUSH() do {} while (0)

DI void signal(const Ctx& x, int lane, int word) {
  DRAIN();
  if (lane == 0) st64(x, u32x2{x.tag0, x.tag1}, 0, WS_FLAGS + word * FSTR);
}
#define POLL_PRE 14
#define PRE_X POLL_PRE
#define PRE_OA POLL_PRE
#define PRE_OB POLL_PRE
#define PRE_KV 0
#define POLL_AUX 16
#define POLL_GAP 0
template <int PRE = POLL_PRE>
DI void poll(const Ctx& x, int word) {
  if (PRE) __builtin_amdgcn_s_sleep(PRE);
  for (unsigned spins = 0; spins < SPIN_CAP; ++spins) {
    asm volatile("" ::: "memory");
    const u32x2 v = __builtin_amdgcn_raw_buffer_load_b64(x.rs, word * FSTR, WS_FLAGS, POLL_AUX);
    if (__all(v.x == x.tag0 && v.y == x.tag1)) break;
    if (POLL_GAP) __builtin_amdgcn_s_sleep(POLL_GAP);
  }
  __builtin_amdgcn_fence(__ATOMIC_ACQUIRE, "wavefront");
  asm volatile("" ::: "memory");
}

DI u32x4 ld_stats(const Ctx& x, int voff, int soff) {
  const u32x2 a = __builtin_amdgcn_raw_buffer_load_b64(x.rs, voff, soff, AUX_SC1), b = __builtin_amdgcn_raw_buffer_load_b64(x.rs, voff + 128, soff, AUX_SC1);
  return u32x4{a.x, a.y, b.x, b.y};
}
DI void finish_stats(u32x4 p, float& mean, float& rstd) {
  float s1 = __uint_as_float(p.x) + __uint_as_float(p.z), s2 = __uint_as_float(p.y) + __uint_as_float(p.w);
  xsum2(s1, s2);
  mean = s1 * (1.f / 512.f);
  float var = fmaxf(s2 * (1.f / 512.f) - mean * mean, 0.f);
  rstd = __builtin_amdgcn_rsqf(var + 1e-5f);
}
DI u32x2 ln_pack(f32x4 a, f32x4 cs, float mean, float rstd) {
  float v0 = fmaf(-mean, cs.x, a.x) * rstd, v1 = fmaf(-mean, cs.y, a.y) * rstd;
  float v2 = fmaf(-mean, cs.z, a.z) * rstd, v3 = fmaf(-mean, cs.w, a.w) * rstd;
  return u32x2{pk2(v0, v1), pk2(v2, v3)};
}

DI void feature_map(const u32x2 (&in)[2], const u32x2 (&wp)[2], unsigned (&o)[8]) {
  f32x4 p[4];
  const f32x4 z = {0.f, 0.f, 0.f, 0.f};
#pragma unroll
  for (int h = 0; h < 2; ++h) {
    p[2 * h] = mfma16(wp[h], in[h], z);
    p[2 * h + 1] = mfma16(u32x2{wp[h].x ^ 0x80008000u, wp[h].y ^ 0x80008000u}, in[h], z);
  }
  float v[16];
#pragma unroll
  for (int i = 0; i < 4; ++i)
#pragma unroll
    for (int r = 0; r < 4; ++r) v[4 * i + r] = p[i][r];
#pragma unroll
  for (int r = 0; r < 4; ++r) { swap32f(v[r], v[8 + r]); swap32f(v[4 + r], v[12 + r]); }
#pragma unroll
  for (int r = 0; r < 4; ++r) { swap16f(v[r], v[4 + r]); swap16f(v[8 + r], v[12 + r]); }
  const float m = max3(max3(max3(v[0], v[1], v[2]), max3(v[3], v[4], v[5]), max3(v[6], v[7], v[8])),
                       max3(max3(v[9], v[10], v[11]), max3(v[12], v[13], v[14]), v[15]), v[15]);
  const float LOG2E = 1.4426950408889634f;
  const float mm = -m * LOG2E;
#pragma unroll
  for (int i = 0; i < 16; ++i) v[i] = __builtin_amdgcn_exp2f(fmaf(v[i], LOG2E, mm));
  f32x2 s2 = {0.f, 0.f};
#pragma unroll
  for (int i = 0; i < 16; i += 4) s2 += (f32x2{v[i], v[i + 1]} + f32x2{v[i + 2], v[i + 3]});
  const float rs = __builtin_amdgcn_rcpf(s2.x + s2.y);
#pragma unroll
  for (int i = 0; i < 8; ++i) { const f32x2 t = f32x2{v[2 * i], v[2 * i + 1]} * rs; o[i] = pk2(t.x, t.y); }
}

#define XWIN 6
#define OWIN 8
#define START_SLEEP 4
#define DMA_SLEEP 1

#define WFRAG(s, ct) (*(LAS u32x4*)((ct) < 2 ? lds_at(wq) + (s) * 2048 + (ct) * 1024 : lds_at(wa) + (s) * 4096 + ((ct) - 2) * 1024))
template <int L>
DI void gemm_phase(const Ctx& x, f32x2 cskeep, u32x4 (&xf)[16], f32x4 (&acc)[6], float& mean, float& rstd, u32x4& hsp, unsigned char* wsp) {
  constexpr bool IS_B = L & 1;
  constexpr int KVW = IS_B ? LDS_Y : LDS_X, SCR = IS_B ? LDS_X : LDS_Y;
  constexpr int LDS_HF = SCR + 4096, LDS_S = SCR + 4096 + 64;
  int lane = x.lane, w = x.wave, gi = x.gi, gj = x.gj;
  OPAQUE_V(lane); OPAQUE_S(w); OPAQUE_S(gi); OPAQUE_S(gj);
  const int c = lane & 15, g = lane >> 4;
  const int LDS_CS = SCR + w * 512;
  const int tau = 8 * gi + w;
  const bool has_halo = IS_B && gi > 0;
  constexpr int KIM = LDS_S, VIMB = KIM + 8192, HP = VIMB + 10240;
  const int wa = KVW + lane * 16, wq = LDS_Z + lane * 16;
  constexpr int C0 = IS_B ? 0 : 2;
  *(LAS float*)lds_at(LDS_CS + lane * 4) = cskeep.x;
  *(LAS float*)lds_at(LDS_CS + 256 + lane * 4) = cskeep.y;
  STAMP(4 + L * 12 + 0);
  int fo = ((EDGE_X + L) * 128 + tau) * 16 + (lane & 7);
  if (has_halo && lane >= 8 && lane < 24) fo = ((EDGE_X + L) * 128 + 8 * gi - 2 + ((lane >> 3) & 1)) * 16 + (w < 2 ? (lane & 7) : w);
  poll<(L == 1 ? PRE_X / 2 : PRE_X)>(x, fo);
  STAMP(4 + L * 12 + 1);
  constexpr int XW = XWIN, XB = L;
  const int xbase = WS_XF + XB * FRAG_BUF + tau * 16384;
#define LDX(s) ld128(x, lane * 16 + ((s) & 3) * 1024, xbase + ((s) >> 2) * 4096)
  u32x4 hx[2], sp;
  const int hc = 8 + (c & 7);
#pragma unroll
  for (int s = 0; s < XW; ++s) {
    xf[s] = LDX(s);
    if (IS_B && s == 2) {
#pragma unroll
      for (int i = 0; i < 2; ++i)
        hx[i] = ld128(x, has_halo ? (8 * gi - 2 + (c >> 3)) * 16384 + (g * 16 + hc) * 16 : 0x7ffffff0, WS_XF + L * FRAG_BUF + w * 2048 + i * 1024);
    }
    if (s == 5) sp = ld_stats(x, g * 256 + c * 8, WS_XS + XB * XS_BUF + tau * 1024);
    SB();
  }
  if (IS_B && w < 2) hsp = ld_stats(x, has_halo ? (8 * gi - 2 + w) * 1024 + g * 256 + hc * 8 : 0x7fffff00, WS_XS + L * XS_BUF);
  u32x4 hf[4];
  f32x4 hacc[4];
#pragma unroll
  for (int ct = 0; ct < 6; ++ct) acc[ct] = f32x4{0.f, 0.f, 0.f, 0.f};
  u32x4 wf[2][6];
#pragma unroll
  for (int ct = C0; ct < 6; ++ct) wf[0][ct] = WFRAG(0, ct);
  SB();
#pragma unroll
  for (int s = 0; s < 16; ++s) {
    if (s + XW < 16) xf[s + XW] = LDX(s + XW);
    if (s + 1 < 16) {
#pragma unroll
      for (int ct = C0; ct < 6; ++ct) wf[(s + 1) & 1][ct] = WFRAG(s + 1, ct);
    }
#pragma unroll
    for (int ct = C0; ct < 6; ++ct) acc[ct] = mfma32(wf[s & 1][ct], xf[s], acc[ct]);
    if (IS_B) {
      if (s == 3 || s == 4) {
#pragma unroll
        for (int i = 0; i < 4; ++i) hacc[i] = mfma32(hf[i], hx[s - 3], s == 3 ? f32x4{0.f, 0.f, 0.f, 0.f} : hacc[i]);
      }
      if (s == 2 || s == 3) {
#pragma unroll
        for (int i = 0; i < 4; ++i) hf[i] = *(LAS u32x4*)(lds_at(wa + w * 8192) + (s - 2) * 4096 + i * 1024);
      }
      if (s == 5) {
#pragma unroll
        for (int i = 0; i < 4; ++i) *(LAS f32x4*)(lds_at(HP + w * 4096 + lane * 16) + i * 1024) = hacc[i];
      }
      if (s == 7) *(volatile LAS u32x2*)lds_at(LDS_HF + w * 8) = u32x2{x.tag0, x.tag1 + L};
    }
    if (s == 10) finish_stats(sp, mean, rstd);
    if (s + 1 < 16) {
#pragma unroll
      for (int i = C0; i < 6; ++i) {
        __builtin_amdgcn_sched_group_barrier(0x008, 1, 0);
        __builtin_amdgcn_sched_group_barrier(0x100, 1, 0);
        if (s == 10) __builtin_amdgcn_sched_group_barrier(0x002, 5, 0);
        if (IS_B && s == 5) __builtin_amdgcn_sched_group_barrier(0x200, 1, 0);
      }
      if (IS_B && s >= 2 && s <= 4) {
#pragma unroll
        for (int i = 0; i < 4; ++i) {
          __builtin_amdgcn_sched_group_barrier(0x008, 1, 0);
          __builtin_amdgcn_sched_group_barrier(0x100, 1, 0);
        }
      }
    }
    SB();
  }
#undef LDX
}

template <int L, class F>
DI void mix_phase(const Ctx& x, const u32x2 (&wp)[2], u32x4 (&po)[8], const u32x4 (&xf)[16], f32x4 (&acc)[6], float mean, float rstd, u32x4 hsp,
                  unsigned char* wsp, F&& late_stores) {
  constexpr bool IS_B = L & 1;
  constexpr int KVW = IS_B ? LDS_Y : LDS_X, SCR = IS_B ? LDS_X : LDS_Y;
  constexpr int LDS_HF = SCR + 4096, LDS_S = SCR + 4096 + 64;
  int lane = x.lane, w = x.wave, gi = x.gi, gj = x.gj;
  OPAQUE_V(lane); OPAQUE_S(w); OPAQUE_S(gi); OPAQUE_S(gj);
  const int c = lane & 15, g = lane >> 4;
  const int LDS_CS = SCR + w * 512;
  const int tau = 8 * gi + w;
  const bool has_halo = IS_B && gi > 0;
  constexpr int KIM = LDS_S, VIMB = KIM + 8192, HP = VIMB + 10240;
  const int wa = KVW + lane * 16, wq = LDS_Z + lane * 16;
  constexpr int C0 = IS_B ? 0 : 2;
  const int q = c >> 2, p = c & 3;
  const u32x4 ones = {0x3f803f80u, 0x3f803f80u, 0x3f803f80u, 0x3f803f80u};
  f32x4 cs[6];
#pragma unroll
  for (int ct = C0; ct < 6; ++ct) cs[ct] = *(LAS f32x4*)(lds_at(LDS_CS + g * 16) + ct * 64);
  STAMP(4 + L * 12 + 2);
  const int obase = WS_OF + L * FRAG_BUF + tau * 16384 + gj * 1024;
#define STAGE_WO() do { _Pragma("unroll") for (int n = 0; n < 8; ++n) *(LAS u32x4*)(lds_at(KVW + w * 8192 + lane * 16) + n * 1024) = po[n]; } while (0)
  if (!IS_B) {
    constexpr int PHI = LDS_S, VIM = PHI + 16384, KVOP = VIM + 8192, ZOP = KVOP + 2048;
    {
      const int row = w * 16 + c;
      u32x2 kn[2];
#pragma unroll
      for (int h = 0; h < 2; ++h) kn[h] = ln_pack(acc[2 + h], cs[2 + h], mean, rstd);
      unsigned ph[8];
      feature_map(kn, wp, ph);
#pragma unroll
      for (int h = 0; h < 2; ++h) *(LAS u32x2*)lds_at(VIM + h * 4096 + row * 32 + g * 8) = ln_pack(acc[4 + h], cs[4 + h], mean, rstd);
      *(LAS u32x4*)lds_at(PHI + g * 4096 + row * 32) = u32x4{ph[0], ph[1], ph[2], ph[3]};
      *(LAS u32x4*)lds_at(PHI + g * 4096 + row * 32 + 16) = u32x4{ph[4], ph[5], ph[6], ph[7]};
    }
    BAR();
    const int e = L >> 1;
    if (w < 4) {
      f32x4 kv0 = {0.f, 0.f, 0.f, 0.f}, kv1 = kv0, z0 = kv0, z1 = kv0;
      LAS unsigned char* pa = lds_at(PHI + w * 4096 + (4 * g + q) * 32 + p * 8);
      LAS unsigned char* pb = lds_at(VIM + (w >> 1) * 4096 + (4 * g + q) * 32 + p * 8);
#pragma unroll
      for (int u = 0; u < 4; u += 2) {
        const u32x4 a0 = tr_frag(pa + u * 1024, 512), b0 = tr_frag(pb + u * 1024, 512);
        const u32x4 a1 = tr_frag(pa + u * 1024 + 1024, 512), b1 = tr_frag(pb + u * 1024 + 1024, 512);
        kv0 = mfma32(a0, b0, kv0); kv1 = mfma32(a1, b1, kv1);
        z0 = mfma32(a0, ones, z0); z1 = mfma32(a1, ones, z1);
      }
      kv0 += kv1; z0 += z1;
      st128(x, __builtin_bit_cast(u32x4, kv0), lane * 16, WS_KV + e * KV_BUF + ((gj * 16 + gi) * 4 + w) * 1024);
      if (c == 0) st128(x, __builtin_bit_cast(u32x4, z0), g * 16, WS_KZ + e * KZ_BUF + (gj * 4 + w) * 1024 + gi * 64);
      if (L > 0) STAGE_WO();
      signal(x, lane, (e * 64 + gj * 4 + w) * 16 + gi);
      if (L == 0) late_stores();
    } else {
      __builtin_amdgcn_s_setprio(1);
    }
    if (L == 0) {
#pragma unroll
      for (int n = 0; n < 8; ++n) po[n] = ld128(x, lane * 16 + (n & 3) * 1024, WS_WOT + gj * 65536 + w * 8192 + (n >> 2) * 4096);
      SB();
    }
    STAMP(4 + L * 12 + 3);
    u32x4 pq[2];
    {
#pragma unroll
      for (int s = 0; s < 16; s += 4) {
        u32x4 f[8];
#pragma unroll
        for (int i = 0; i < 8; ++i) f[i] = WFRAG(s + (i >> 1), i & 1);
#pragma unroll
        for (int i = 0; i < 8; ++i) acc[i & 1] = mfma32(f[i], xf[s + (i >> 1)], acc[i & 1]);
        SB();
      }
      u32x2 qn[2];
#pragma unroll
      for (int h = 0; h < 2; ++h) qn[h] = ln_pack(acc[h], *(LAS f32x4*)(lds_at(LDS_CS + g * 16) + h * 64), mean, rstd);
      unsigned ph[8];
      feature_map(qn, wp, ph);
#pragma unroll
      for (int r = 0; r < 2; ++r) { swap16(ph[r], ph[2 + r]); swap16(ph[4 + r], ph[6 + r]); }
#pragma unroll
      for (int r = 0; r < 2; ++r) { swap32(ph[r], ph[4 + r]); swap32(ph[2 + r], ph[6 + r]); }
      pq[0] = u32x4{ph[0], ph[1], ph[2], ph[3]};
      pq[1] = u32x4{ph[4], ph[5], ph[6], ph[7]};
      OPAQUE_V(pq[0]); OPAQUE_V(pq[1]);
    }
    STAMP(4 + L * 12 + 4);
    if (w >= 4) {
      const int wi = w - 4;
      poll<PRE_KV>(x, (e * 64 + gj * 4 + wi) * 16 + c);
      STAMP(4 + L * 12 + 5);
      const int sbase = WS_KV + e * KV_BUF + (gj * 64 + wi) * 1024;
      const u32x4 zu = ld128(x, lane * 16, WS_KZ + e * KZ_BUF + (gj * 4 + wi) * 1024);
      u32x4 a[16];
#pragma unroll
      for (int i = 0; i < 16; ++i) a[i] = ld128(x, lane * 16, sbase + i * 4096);
      SB();
      STAGE_WO();
      const f32x4 zs = sum16q(zu);
      if (lane < 4) *(LAS u32x2*)lds_at(ZOP + (wi >> 1) * 64 + lane * 16 + (wi & 1) * 8) = u32x2{pk2(zs.x, zs.y), pk2(zs.z, zs.w)};
      f32x4 t[4];
#pragma unroll
      for (int i = 0; i < 4; ++i)
        t[i] = (__builtin_bit_cast(f32x4, a[4 * i]) + __builtin_bit_cast(f32x4, a[4 * i + 1])) + (__builtin_bit_cast(f32x4, a[4 * i + 2]) + __builtin_bit_cast(f32x4, a[4 * i + 3]));
      const f32x4 kv = (t[0] + t[1]) + (t[2] + t[3]);
      *(LAS u32x2*)lds_at(KVOP + (wi >> 1) * 1024 + lane * 16 + (wi & 1) * 8) = u32x2{pk2(kv.x, kv.y), pk2(kv.z, kv.w)};
      __builtin_amdgcn_s_setprio(0);
    } else if (L == 0) {
      if (w < 3) signal(x, lane, (EDGE_SETUP * 128 + 64 + gj * 4 + w) * 16 + gi);
      STAGE_WO();
    }
    BAR();
    {
      const f32x4 z = {0.f, 0.f, 0.f, 0.f};
      unsigned o[4];
#pragma unroll
      for (int h = 0; h < 2; ++h) {
        const u32x4 kvop = *(LAS u32x4*)lds_at(KVOP + h * 1024 + lane * 16);
        const u32x4 zop = *(LAS u32x4*)lds_at(ZOP + h * 64 + g * 16);
        f32x4 num = mfma32(kvop, pq[h], z), den = mfma32(zop, pq[h], z);
        const float r = __builtin_amdgcn_rcpf(den.x + 1e-6f);
        o[2 * h] = pk2(num.x * r, num.y * r); o[2 * h + 1] = pk2(num.z * r, num.w * r);
      }
      st128(x, u32x4{o[0], o[1], o[2], o[3]}, lane * 16, obase);
    }
  } else {
    constexpr int VIM = VIMB;
    u32x2 qn[2], kn[2], kp[2];
#pragma unroll
    for (int h = 0; h < 2; ++h) {
      kn[h] = ln_pack(acc[2 + h], cs[2 + h], mean, rstd);
      *(LAS u32x2*)lds_at(KIM + (w * 2 + h) * 512 + lane * 8) = kn[h];
      *(LAS u32x2*)lds_at(VIM + ((2 + w) * 2 + h) * 512 + c * 32 + g * 8) = ln_pack(acc[4 + h], cs[4 + h], mean, rstd);
    }
    if (w < 2) {
      for (unsigned spins = 0; spins < SPIN_CAP; ++spins) {
        const u32x2 v = *(volatile LAS u32x2*)lds_at(LDS_HF + (lane & 7) * 8);
        if (__all(v.x == x.tag0 && v.y == x.tag1 + L)) break;
      }
      float hm, hr;
      finish_stats(hsp, hm, hr);
      const int src = HP + (c < 8 ? 2048 : 0) + (g * 16 + w * 8 + (c & 7)) * 16;
#pragma unroll
      for (int h = 0; h < 2; ++h) {
        f32x4 a[8];
#pragma unroll
        for (int i = 0; i < 8; ++i) a[i] = *(LAS f32x4*)(lds_at(src) + i * 4096 + h * 1024);
        const f32x4 sum = ((a[0] + a[1]) + (a[2] + a[3])) + ((a[4] + a[5]) + (a[6] + a[7]));
        const f32x4 csw = c < 8 ? cs[4 + h] : cs[2 + h];
        u32x2 v = ln_pack(sum, csw, hm, hr);
        if (!has_halo) v = u32x2{0u, 0u};
        kp[h] = c < 8 ? u32x2{0u, 0u} : v;
        *(LAS u32x2*)lds_at(VIM + (w * 2 + h) * 512 + (c ^ 8) * 32 + g * 8) = c < 8 ? v : u32x2{0u, 0u};
      }
    }
#pragma unroll
    for (int h = 0; h < 2; ++h) qn[h] = ln_pack(acc[h], cs[h], mean, rstd);
    const float NEG = -1e30f;
    f32x4 bo, bp;
    const bool noprev = (w < 2) && !has_halo;
#pragma unroll
    for (int r = 0; r < 4; ++r) {
      const int key = 4 * g + r;
      bo[r] = (c - key >= 0 && c - key <= 7) ? 0.f : NEG;
      bp[r] = (key >= c + 9 && !noprev) ? 0.f : NEG;
    }
    f32x4 so[2];
#pragma unroll
    for (int h = 0; h < 2; ++h) so[h] = mfma16(kn[h], qn[h], bo);
    BAR();
    {
      const f32x4 z = {0.f, 0.f, 0.f, 0.f};
      const float SC = 0.25f * 1.4426950408889634f;
      f32x4 sp_[2];
      u32x2 vo[2], vp[2];
      float m[2];
#pragma unroll
      for (int h = 0; h < 2; ++h) {
        if (w >= 2) kp[h] = *(LAS u32x2*)lds_at(KIM + ((w - 2) * 2 + h) * 512 + lane * 8);
        vp[h] = tr_read(lds_at(VIM + (w * 2 + h) * 512 + (4 * g + q) * 32 + p * 8));
        vo[h] = tr_read(lds_at(VIM + ((2 + w) * 2 + h) * 512 + (4 * g + q) * 32 + p * 8));
      }
#pragma unroll
      for (int h = 0; h < 2; ++h) {
        sp_[h] = mfma16(kp[h], qn[h], bp);
        m[h] = max3(max3(so[h].x, so[h].y, so[h].z), max3(so[h].w, sp_[h].x, sp_[h].y), max3(sp_[h].z, sp_[h].w, sp_[h].w));
      }
      swap32f(m[0], m[1]);
      float t = max3(m[0], m[1], m[1]), u = t;
      swap16f(t, u);
      float a = max3(t, u, u), b = a;
      swap32f(a, b);
      m[0] = a; m[1] = b;
      unsigned o[4];
#pragma unroll
      for (int h = 0; h < 2; ++h) {
        const float mm = -m[h] * SC;
        float e[8];
#pragma unroll
        for (int r = 0; r < 4; ++r) { e[r] = __builtin_amdgcn_exp2f(fmaf(so[h][r], SC, mm)); e[4 + r] = __builtin_amdgcn_exp2f(fmaf(sp_[h][r], SC, mm)); }
        const u32x4 P = {pk2(e[0], e[1]), pk2(e[2], e[3]), pk2(e[4], e[5]), pk2(e[6], e[7])};
        f32x4 num = mfma32(u32x4{vo[h].x, vo[h].y, vp[h].x, vp[h].y}, P, z), den = mfma32(ones, P, z);
        const float r = __builtin_amdgcn_rcpf(den.x);
        o[2 * h] = pk2(num.x * r, num.y * r); o[2 * h + 1] = pk2(num.z * r, num.w * r);
      }
      st128(x, u32x4{o[0], o[1], o[2], o[3]}, lane * 16, obase);
    }
    SB();
    STAGE_WO();
  }
  STAMP(4 + L * 12 + 6);
  signal(x, lane, ((EDGE_O + L) * 128 + tau) * 16 + gj);
  BAR();
#undef WFRAG
#undef STAGE_WO
}

template <int L>
DI void oproj_phase(const Ctx& x, f32x4 (&res)[4], u32x4 (&poB)[8], f32x2& csB, u16* out, unsigned char* wsp) {
  constexpr bool IS_B = L & 1;
  constexpr int KVW = IS_B ? LDS_Y : LDS_X, SCR = IS_B ? LDS_X : LDS_Y;
  int lane = x.lane, w = x.wave, gi = x.gi, gj = x.gj;
  OPAQUE_V(lane); OPAQUE_S(w); OPAQUE_S(gi); OPAQUE_S(gj);
  const int c = lane & 15, g = lane >> 4;
  const int tau = 8 * gi + 4 * (gj >> 3) + w;
  STAMP(4 + L * 12 + 7);
  f32x4 cb[2] = {f32x4{0.f, 0.f, 0.f, 0.f}, f32x4{0.f, 0.f, 0.f, 0.f}};
  if (w < 4) {
    const int wb = KVW + lane * 16;
    constexpr int PD = 3, OW = OWIN;
    u32x4 wf[PD][4];
#pragma unroll
    for (int s = 0; s < PD; ++s)
#pragma unroll
      for (int ct = 0; ct < 4; ++ct) wf[s][ct] = *(LAS u32x4*)(lds_at(wb) + s * 4096 + ct * 1024);
    poll<(IS_B ? PRE_OB : PRE_OA)>(x, ((EDGE_O + L) * 128 + tau) * 16 + c);
    STAMP(4 + L * 12 + 8);
    const int obase = WS_OF + L * FRAG_BUF + tau * 16384;
    u32x4 of[16];
#pragma unroll
    for (int s = 0; s < OW; ++s) { of[s] = ld128(x, lane * 16 + (s & 3) * 1024, obase + (s >> 2) * 4096); SB(); }
#pragma unroll
    for (int s = 0; s < 16; ++s) {
      if (s + OW < 16) of[s + OW] = ld128(x, lane * 16 + ((s + OW) & 3) * 1024, obase + ((s + OW) >> 2) * 4096);
#pragma unroll
      for (int ct = 0; ct < 4; ++ct) {
        res[ct] = mfma32(wf[s % PD][ct], of[s], res[ct]);
        if (s + PD < 16) wf[s % PD][ct] = *(LAS u32x4*)(lds_at(wb) + (s + PD) * 4096 + ct * 1024);
      }
      SB();
    }
    STAMP(4 + L * 12 + 9);
    const int nb = gj & 7;
    u32x4 f[2];
#pragma unroll
    for (int a = 0; a < 2; ++a)
      f[a] = u32x4{pk2(res[2 * a].x, res[2 * a].y), pk2(res[2 * a].z, res[2 * a].w), pk2(res[2 * a + 1].x, res[2 * a + 1].y), pk2(res[2 * a + 1].z, res[2 * a + 1].w)};
    if (L < 3) {
#pragma unroll
      for (int a = 0; a < 2; ++a) st128(x, f[a], lane * 16 + a * 1024, WS_XF + (L + 1) * FRAG_BUF + (tau * 16 + nb * 2) * 1024);
      const f32x4 s = (res[0] + res[1]) + (res[2] + res[3]);
      const f32x4 qq = (res[0] * res[0] + res[1] * res[1]) + (res[2] * res[2] + res[3] * res[3]);
      float s1 = (s.x + s.y) + (s.z + s.w), s2 = (qq.x + qq.y) + (qq.z + qq.w);
      xsum2(s1, s2);
      if (lane < 16) st64(x, u32x2{__float_as_uint(s1), __float_as_uint(s2)}, lane * 8, WS_XS + (L + 1) * XS_BUF + tau * 1024 + nb * 128);
      signal(x, lane, ((EDGE_X + L + 1) * 128 + tau) * 16 + nb);
    } else {
      const int tok = 32 * (tau >> 1) + 2 * c + (tau & 1);
#pragma unroll
      for (int a = 0; a < 2; ++a) {
        *(GAS u32x4*)(out + tok * 512 + nb * 64 + a * 32 + 8 * g) = f[a];
      }
    }
    STAMP(4 + L * 12 + 10);
  } else if (L < 3) {
    if (!IS_B && L == 0) {
      poll<0>(x, (EDGE_SETUP * 128 + 64 + gj * 4) * 16 + (lane < 48 ? lane : 0));
#pragma unroll
      for (int n = 0; n < 2; ++n) cb[n] = sum16q(ld128(x, lane * 16, WS_CSP + (16 + gj) * 6144 + ((w < 7 ? w - 4 : 0) * 2 + n) * 1024));
    }
    const int src = WS_WT + ((IS_B ? 0 : 16) + gj) * 98304;
    const int wi = w - 4;
    u32x4 f[24];
#define FSRC(n) ((n) < 16 ? src + wi * 16384 + ((n) >> 2) * 4096 : src + 65536 + wi * 8192 + (((n) - 16) >> 2) * 4096)
#define FDST(n) ((n) < 16 ? lds_at(SCR + wi * 16384 + lane * 16) + (n) * 1024 : lds_at(LDS_Z + wi * 8192 + lane * 16) + ((n) - 16) * 1024)
#pragma unroll
    for (int n = 0; n < 24; ++n) {
      f[n] = ld128(x, lane * 16 + (n & 3) * 1024, FSRC(n));
      __builtin_amdgcn_s_sleep(DMA_SLEEP);
      if (n >= 6) *(LAS u32x4*)FDST(n - 6) = f[n - 6];
      SB();
    }
#pragma unroll
    for (int n = 18; n < 24; ++n) *(LAS u32x4*)FDST(n) = f[n];
#undef FSRC
#undef FDST
    STAMP(4 + L * 12 + 10);
  }
  if (!IS_B && L == 0) {
    if (w >= 4) {
#pragma unroll
      for (int n = 0; n < 8; ++n) poB[n] = ld128(x, lane * 16 + (n & 3) * 1024, WS_WOT + (16 + gj) * 65536 + w * 8192 + (n >> 2) * 4096);
    }
    BAR();
    if (w < 4) {
#pragma unroll
      for (int n = 0; n < 8; ++n) poB[n] = ld128(x, lane * 16 + (n & 3) * 1024, WS_WOT + (16 + gj) * 65536 + w * 8192 + (n >> 2) * 4096);
    }
    if (w >= 4 && w < 7 && lane < 4) {
#pragma unroll
      for (int n = 0; n < 2; ++n) *(LAS f32x4*)lds_at(KVW + 61440 + ((w - 4) * 2 + n) * 64 + lane * 16) = cb[n];
    }
    BAR();
    csB = f32x2{*(LAS float*)lds_at(KVW + 61440 + lane * 4), *(LAS float*)lds_at(KVW + 61440 + 256 + (lane & 31) * 4)};
  } else
  if (L < 3) BAR();
}

__global__ void __launch_bounds__(512, 2)
fused_fwd(const u16* __restrict__ x0, const u16* __restrict__ wA, const u16* __restrict__ wphi, const u16* __restrict__ woA,
          const u16* __restrict__ wB, const u16* __restrict__ woB, u16* __restrict__ out, unsigned char* __restrict__ wsp) {
  extern __shared__ __attribute__((aligned(16))) unsigned char smem[];
  asm volatile("" : "+s"(x0), "+s"(wA), "+s"(wphi), "+s"(woA), "+s"(wB), "+s"(woB), "+s"(out), "+s"(wsp));
  Ctx x;
  x.lane = threadIdx.x & 63;
  x.wave = __builtin_amdgcn_readfirstlane(threadIdx.x >> 6);
  {
    constexpr int NJ = MAP_NJ, NI = 32 / NJ, XJ = 16 / NJ;
    const int xc = blockIdx.x & 7, r = blockIdx.x >> 3;
    x.gi = (xc / XJ) * NI + r / NJ;
    x.gj = (xc % XJ) * NJ + r % NJ;
  }
  x.rs = __builtin_amdgcn_make_buffer_rsrc((void*)wsp, 0, WS_END, 0x00020000);
  STAMP(0);
  const int lane = x.lane, w = x.wave, c = lane & 15, g = lane >> 4, q = c >> 2, p = c & 3, gi = x.gi, gj = x.gj;
  const u32x4 ones = {0x3f803f80u, 0x3f803f80u, 0x3f803f80u, 0x3f803f80u};
  const int tau0 = 8 * gi + w;
  const int xstg = (w < 4 ? LDS_Y : LDS_X) + (w & 3) * 16384;
  f32x4 res[4];
  auto unit_dma = [&](int u, int slot) {
    const bool isw = u < 6;
    const char* src = isw ? (const char*)(u < 3 ? wA : wB) + (size_t)gi * 98304 + (u % 3) * 1024 + gj * 64
                          : (const char*)(u < 8 ? woA : woB) + (size_t)gi * 32768 + (gj & 7) * 128 + (u & 1) * 64;
    const int ld = isw ? 3072 : 1024;
    const unsigned vo = (lane >> 2) * ld + (lane & 3) * 16;
    dma16(src, vo, slot);
    dma16(src + 16 * ld, vo, slot + 1024);
  };
  auto unit_read = [&](int u, int slot, u32x4 (&f)[2]) {
    const bool isw = u < 6;
    const int ad = slot + (isw ? (8 * g + q) * 64 + p * 8 : (4 * g + q) * 64 + p * 16);
    const int sec = isw ? 256 : 1024, astr = isw ? 32 : 8;
#pragma unroll
    for (int a = 0; a < 2; ++a) {
      const u32x2 lo = tr_read(lds_at(ad + a * astr)), hi = tr_read(lds_at(ad + a * astr + sec));
      f[a] = u32x4{lo.x, lo.y, hi.x, hi.y};
    }
  };
  auto unit_store = [&](int u, const u32x4 (&f)[2]) {
    const bool isw = u < 6;
    const int s3 = u % 3;
    const int dst = isw ? WS_WT + ((u / 3) * 16 + gj) * 98304 + (s3 == 0 ? 65536 + gi * 2048 : gi * 4096 + (s3 - 1) * 2048)
                        : WS_WOT + (((u - 6) >> 1) * 16 + gj) * 65536 + gi * 4096 + (u & 1) * 2048;
#pragma unroll
    for (int a = 0; a < 2; ++a) st128(x, f[a], lane * 16 + a * 1024, dst);
    if (isw) {
      const f32x4 s0 = mfma32(f[0], ones, f32x4{0.f, 0.f, 0.f, 0.f}), s1 = mfma32(f[1], ones, f32x4{0.f, 0.f, 0.f, 0.f});
      if (c < 2) st128(x, __builtin_bit_cast(u32x4, c ? s1 : s0), g * 16 + c * 1024, WS_CSP + (((u / 3) * 16 + gj) * 6 + s3 * 2) * 1024 + gi * 64);
    }
  };
  auto load_tag = [&]() {
    unsigned long long t;
    asm volatile("s_load_dwordx2 %0, %1, 0x0\n\ts_waitcnt lgkmcnt(0)" : "=s"(t) : "s"(wsp + WS_EPOCH) : "memory");
    t += 1ull;
    x.tag0 = (unsigned)t; x.tag1 = (unsigned)(t >> 32);
  };
  const int zs = LDS_Z + (w & 3) * 8192;
  if (w < 4) {
    const int u0 = w < 3 ? w : 6;
    unit_dma(u0, zs);
    if (w == 3) unit_dma(7, zs + 2048);
    load_tag();
    DRAIN();
    STAMP(52);
    u32x4 f[2];
    unit_read(u0, zs, f);
    unit_store(u0, f);
    if (w == 3) { unit_read(7, zs + 2048, f); unit_store(7, f); }
    signal(x, lane, (EDGE_SETUP * 128 + gj * 4 + w) * 16 + gi);
  } else {
    __builtin_amdgcn_s_sleep(START_SLEEP);
  }
  STAMP(53);
  {
    const char* xrow = (const char*)x0 + (32 * (tau0 >> 1) + (tau0 & 1)) * 1024;
#pragma unroll
    for (int n = 0; n < 16; ++n) dma16(xrow + n * 2048, (lane ^ (n << 2)) << 4, xstg + n * 1024);
  }
  if (w < 4) {
    if (w < 3) unit_dma(3 + w, zs + 2048);
    if (w < 2) unit_dma(8 + w, zs + 4096);
    const int tau = 8 * gi + 4 * (gj >> 3) + w;
    const unsigned rvo = (32 * (tau >> 1) + 2 * (lane >> 3) + (tau & 1)) * 1024 + ((lane & 7) << 4);
    dma16((const char*)x0 + (gj & 7) * 128, rvo, zs + 6144);
    dma16((const char*)x0 + (gj & 7) * 128 + 16384, rvo, zs + 7168);
  }
  STAMP(56);
  u32x2 wp[2];
#pragma unroll
  for (int h = 0; h < 2; ++h) {
    const GAS u16* pw = (const GAS u16*)wphi + ((2 * gj + h) * 16 + 4 * g) * 16 + c;
    const unsigned a0 = pw[0], a1 = pw[16], a2 = pw[32], a3 = pw[48];
    wp[h] = u32x2{a0 | (a1 << 16), a2 | (a3 << 16)};
  }
  if (w >= 4) load_tag();
  DRAIN();
  STAMP(1);
  u32x4 xf[16], fd0[2], fd1[2];
#pragma unroll
  for (int s = 0; s < 16; ++s) xf[s] = *(LAS u32x4*)lds_at(xstg + c * 1024 + ((s ^ c) << 6) + g * 16);
  unit_read(3, zs + 2048, fd0);
  unit_read(8, zs + 4096, fd1);
#pragma unroll
  for (int a = 0; a < 2; ++a) {
    const u32x4 v = *(LAS u32x4*)lds_at(zs + 6144 + c * 128 + ((a * 4 + g) << 4));
    res[2 * a] = f32x4{bflo(v.x), bfhi(v.x), bflo(v.y), bfhi(v.y)};
    res[2 * a + 1] = f32x4{bflo(v.z), bfhi(v.z), bflo(v.w), bfhi(v.w)};
  }
  asm volatile("s_waitcnt lgkmcnt(0)" : "+v"(xf[0]), "+v"(xf[1]), "+v"(xf[2]), "+v"(xf[3]), "+v"(xf[4]), "+v"(xf[5]), "+v"(xf[6]), "+v"(xf[7]),
               "+v"(xf[8]), "+v"(xf[9]), "+v"(xf[10]), "+v"(xf[11]), "+v"(xf[12]), "+v"(xf[13]), "+v"(xf[14]), "+v"(xf[15]),
               "+v"(fd0[0]), "+v"(fd0[1]), "+v"(fd1[0]), "+v"(fd1[1]), "+v"(res[0]), "+v"(res[1]), "+v"(res[2]), "+v"(res[3]) :: "memory");
  STAMP(54);
  float mean, rstd;
  auto row_stats = [&]() {
    f32x4 accs = {0.f, 0.f, 0.f, 0.f}, accq = accs;
#pragma unroll
    for (int n = 0; n < 16; ++n) { accs = mfma32(ones, xf[n], accs); accq = mfma32(xf[n], xf[n], accq); }
    const float dq = sel4(accq, p);
    finish_stats(u32x4{__float_as_uint(accs.x * 0.25f), g == q ? __float_as_uint(dq) : 0u, 0u, 0u}, mean, rstd);
    OPAQUE_V(mean); OPAQUE_V(rstd);
  };
  row_stats();
  u32x4 fq[8];
  f32x4 cb[2];
  if (w >= 4) {
    poll<0>(x, (EDGE_SETUP * 128 + gj * 4) * 16 + lane);
    STAMP(55);
    u32x4 f[16], cp[2];
#pragma unroll
    for (int n = 0; n < 16; ++n) { f[n] = ld128(x, lane * 16 + (n & 3) * 1024, WS_WT + gj * 98304 + (w - 4) * 16384 + (n >> 2) * 4096); SB(); }
#pragma unroll
    for (int n = 0; n < 2; ++n) cp[n] = ld128(x, lane * 16, WS_CSP + gj * 6144 + ((w < 7 ? w - 4 : 0) * 2 + n) * 1024);
#pragma unroll
    for (int n = 0; n < 8; ++n) { fq[n] = ld128(x, lane * 16 + (n & 3) * 1024, WS_WT + gj * 98304 + 65536 + (w - 4) * 8192 + (n >> 2) * 4096); SB(); }
#pragma unroll
    for (int n = 0; n < 16; ++n) *(LAS u32x4*)(lds_at(xstg + lane * 16) + n * 1024) = f[n];
#pragma unroll
    for (int n = 0; n < 2; ++n) cb[n] = sum16q(cp[n]);
  } else {
#pragma unroll
    for (int n = 0; n < 8; ++n) fq[n] = u32x4{0u, 0u, 0u, 0u};
    cb[0] = f32x4{0.f, 0.f, 0.f, 0.f}; cb[1] = cb[0];
  }
  STAMP(2);
  BAR();
  if (w >= 4) {
    if (w < 7 && lane < 4) {
#pragma unroll
      for (int n = 0; n < 2; ++n) *(LAS f32x4*)lds_at(LDS_Y + 61440 + ((w - 4) * 2 + n) * 64 + lane * 16) = cb[n];
    }
    if (w < 7) *(volatile LAS u32x2*)lds_at(LDS_Y + 61952 + (w - 4) * 8) = u32x2{x.tag0, x.tag1};
#pragma unroll
    for (int n = 0; n < 8; ++n) *(LAS u32x4*)(lds_at(LDS_Z + (w - 4) * 8192 + lane * 16) + n * 1024) = fq[n];
  }
  u32x4 poA[8], poB[8];
  f32x4 acc[6];
#pragma unroll
  for (int ct = 0; ct < 6; ++ct) acc[ct] = f32x4{0.f, 0.f, 0.f, 0.f};
  {
    u32x4 wf[2][4];
#pragma unroll
    for (int ct = 0; ct < 4; ++ct) wf[0][ct] = *(LAS u32x4*)(lds_at(LDS_X + lane * 16) + ct * 1024);
    SB();
#pragma unroll
    for (int s = 0; s < 16; ++s) {
      if (s + 1 < 16) {
#pragma unroll
        for (int ct = 0; ct < 4; ++ct) wf[(s + 1) & 1][ct] = *(LAS u32x4*)(lds_at(LDS_X + lane * 16) + (s + 1) * 4096 + ct * 1024);
      }
#pragma unroll
      for (int ct = 0; ct < 4; ++ct) acc[2 + ct] = mfma32(wf[s & 1][ct], xf[s], acc[2 + ct]);
      if (s + 1 < 16) {
#pragma unroll
        for (int i = 0; i < 4; ++i) {
          __builtin_amdgcn_sched_group_barrier(0x008, 1, 0);
          __builtin_amdgcn_sched_group_barrier(0x100, 1, 0);
        }
      }
      SB();
    }
  }
  for (unsigned spins = 0; spins < SPIN_CAP; ++spins) {
    const u32x2 v = *(volatile LAS u32x2*)lds_at(LDS_Y + 61952 + (lane % 3) * 8);
    if (__all(v.x == x.tag0 && v.y == x.tag1)) break;
  }
  const f32x2 csA = {*(volatile LAS float*)lds_at(LDS_Y + 61440 + lane * 4), *(volatile LAS float*)lds_at(LDS_Y + 61440 + 256 + (lane & 31) * 4)};
  *(LAS float*)lds_at(LDS_Y + w * 512 + lane * 4) = csA.x;
  *(LAS float*)lds_at(LDS_Y + w * 512 + 256 + lane * 4) = csA.y;
  f32x2 csB = {0.f, 0.f};
  u32x4 hsp = {0u, 0u, 0u, 0u};
  auto none = []() {};
  mix_phase<0>(x, wp, poA, xf, acc, mean, rstd, hsp, wsp, [&]() {
    if (w < 3) unit_store(3 + w, fd0);
    if (w < 2) unit_store(8 + w, fd1);
  });
  oproj_phase<0>(x, res, poB, csB, out, wsp);
  if (blockIdx.x == 0 && threadIdx.x == 0) st64(x, u32x2{x.tag0, x.tag1}, 0, WS_EPOCH);
  gemm_phase<1>(x, csB, xf, acc, mean, rstd, hsp, wsp);
  mix_phase<1>(x, wp, poB, xf, acc, mean, rstd, hsp, wsp, none);
  oproj_phase<1>(x, res, poB, csB, out, wsp);
  gemm_phase<2>(x, csA, xf, acc, mean, rstd, hsp, wsp);
  mix_phase<2>(x, wp, poA, xf, acc, mean, rstd, hsp, wsp, none);
  oproj_phase<2>(x, res, poB, csB, out, wsp);
  gemm_phase<3>(x, csB, xf, acc, mean, rstd, hsp, wsp);
  mix_phase<3>(x, wp, poB, xf, acc, mean, rstd, hsp, wsp, none);
  oproj_phase<3>(x, res, poB, csB, out, wsp);
  STAMP(3);
  STAMP_FLUSH();
}

extern "C" void kernel_launch(void* const* d_in, const int* in_sizes, int n_in, void* d_out, int out_size, void* d_ws, size_t ws_size,
                              hipStream_t stream) {
  (void)in_sizes; (void)n_in; (void)out_size; (void)ws_size;
  (void)hipFuncSetAttribute((const void*)fused_fwd, hipFuncAttributeMaxDynamicSharedMemorySize, LDS_BYTES);
  fused_fwd<<<256, 512, LDS_BYTES, stream>>>((const u16*)d_in[0], (const u16*)d_in[1], (const u16*)d_in[2], (const u16*)d_in[3],
                                             (const u16*)d_in[5], (const u16*)d_in[6], (u16*)d_out, (unsigned char*)d_ws);
}
```

```cpp
#include <hip/hip_runtime.h>
#include <stdint.h>

typedef unsigned short u16;
typedef __bf16 bf16x8 __attribute__((ext_vector_type(8)));
typedef __bf16 bf16x2 __attribute__((ext_vector_type(2)));
typedef short s16x4 __attribute__((ext_vector_type(4)));
typedef float f32x4 __attribute__((ext_vector_type(4)));
typedef float f32x2 __attribute__((ext_vector_type(2)));
typedef unsigned u32x2 __attribute__((ext_vector_type(2)));
typedef unsigned u32x4 __attribute__((ext_vector_type(4)));
#define LAS __attribute__((address_space(3)))
#define GAS __attribute__((address_space(1)))
#define DI __device__ __forceinline__

constexpr int WS_EPOCH = 0;
constexpr int FSTR = 64;
constexpr int WS_FLAGS = 24 << 20;
constexpr int EDGE_X = 0, EDGE_O = 4, EDGE_SETUP = 8;
constexpr int XS_BUF = 128 * 1024;
constexpr int WS_XS = 262144 - XS_BUF;
constexpr int WS_KZ = WS_XS + 4 * XS_BUF;
constexpr int KZ_BUF = 16 * 4 * 1024;
constexpr int WS_CSP = WS_KZ + 2 * KZ_BUF;
constexpr int FRAG_BUF = 2 << 20;
constexpr int WS_KV = 1 << 20;
constexpr int KV_BUF = 16 * 16 * 4 * 1024;
constexpr int WS_XF = WS_KV + 2 * KV_BUF - FRAG_BUF;
constexpr int WS_OF = WS_XF + 4 * FRAG_BUF;
constexpr int WS_WT = WS_OF + 4 * FRAG_BUF;
constexpr int WS_WOT = WS_WT + 2 * 16 * 98304;
constexpr int WS_END = 28 << 20;
static_assert(WS_WOT + 2 * 16 * 65536 <= WS_FLAGS && WS_FLAGS + 9 * 128 * 16 * FSTR <= WS_END && WS_CSP + 2 * 16 * 6144 <= WS_KV, "workspace map");

constexpr int LDS_X = 0, LDS_Y = 65536, LDS_Z = 131072;
constexpr int LDS_BYTES = 163840;
constexpr int AUX_SC1 = 16;
constexpr int MAP_NJ = 8;
constexpr unsigned SPIN_CAP = 1u << 16;

DI unsigned pk2(float a, float b) { f32x2 v = {a, b}; return __builtin_bit_cast(unsigned, __builtin_convertvector(v, bf16x2)); }
DI float bflo(unsigned u) { return __uint_as_float(u << 16); }
DI float bfhi(unsigned u) { return __uint_as_float(u & 0xffff0000u); }
DI f32x4 mfma32(u32x4 a, u32x4 b, f32x4 c) {
  return __builtin_amdgcn_mfma_f32_16x16x32_bf16(__builtin_bit_cast(bf16x8, a), __builtin_bit_cast(bf16x8, b), c, 0, 0, 0);
}
DI f32x4 mfma16(u32x2 a, u32x2 b, f32x4 c) {
  return __builtin_amdgcn_mfma_f32_16x16x16bf16_1k(__builtin_bit_cast(s16x4, a), __builtin_bit_cast(s16x4, b), c, 0, 0, 0);
}
DI LAS unsigned char* lds_at(int off) { return (LAS unsigned char*)(uintptr_t)(unsigned)off; }
DI u32x2 tr_read(LAS unsigned char* p) { return __builtin_bit_cast(u32x2, __builtin_amdgcn_ds_read_tr16_b64_v4i16((LAS s16x4*)p)); }
DI u32x4 tr_frag(LAS unsigned char* p, int second) {
  u32x2 a = tr_read(p), b = tr_read(p + second);
  return u32x4{a.x, a.y, b.x, b.y};
}
DI void swap32(unsigned& a, unsigned& b) { auto r = __builtin_amdgcn_permlane32_swap(a, b, false, false); a = r[0]; b = r[1]; }
DI void swap16(unsigned& a, unsigned& b) { auto r = __builtin_amdgcn_permlane16_swap(a, b, false, false); a = r[0]; b = r[1]; }
DI void swap32f(float& a, float& b) { unsigned x = __float_as_uint(a), y = __float_as_uint(b); swap32(x, y); a = __uint_as_float(x); b = __uint_as_float(y); }
DI void swap16f(float& a, float& b) { unsigned x = __float_as_uint(a), y = __float_as_uint(b); swap16(x, y); a = __uint_as_float(x); b = __uint_as_float(y); }
DI float max3(float a, float b, float c) { return __builtin_fmaxf(__builtin_fmaxf(a, b), c); }
DI float max2(float a, float b) { float ninf = -__builtin_inff(); asm("" : "+s"(ninf)); return max3(a, b, ninf); }
DI float sel4(f32x4 v, int i) { const float a = (i & 1) ? v.y : v.x, b = (i & 1) ? v.w : v.z; return (i & 2) ? b : a; }
DI float sum16q(float s) {
  s += __builtin_bit_cast(float, __builtin_amdgcn_update_dpp(0, __builtin_bit_cast(int, s), 0x124, 0xf, 0xf, true));
  s += __builtin_bit_cast(float, __builtin_amdgcn_update_dpp(0, __builtin_bit_cast(int, s), 0x128, 0xf, 0xf, true));
  float a = s, b = s;
  swap16f(a, b);
  s = a + b; a = s; b = s;
  swap32f(a, b);
  return a + b;
}
DI f32x4 sum16q(u32x4 v) { return f32x4{sum16q(__uint_as_float(v.x)), sum16q(__uint_as_float(v.y)), sum16q(__uint_as_float(v.z)), sum16q(__uint_as_float(v.w))}; }
DI void xsum2(float& s1, float& s2) {
  swap32f(s1, s2);
  float t = s1 + s2, u = t;
  swap16f(t, u);
  float a = t + u, b = a;
  swap32f(a, b);
  s1 = a; s2 = b;
}
#define BAR() asm volatile("s_waitcnt lgkmcnt(0)\n\ts_barrier" ::: "memory")
#define DRAIN() asm volatile("s_waitcnt vmcnt(0)" ::: "memory")
#define SB() __builtin_amdgcn_sched_barrier(0)
#define OPAQUE_V(x) asm volatile("" : "+v"(x))
#define OPAQUE_S(x) asm volatile("" : "+s"(x))

DI void dma16(const void* sbase, unsigned voff, unsigned lds_addr) {
  unsigned keep;
  asm volatile("s_mov_b32 %0, m0\n\ts_mov_b32 m0, %3\n\ts_nop 2\n\tglobal_load_lds_dwordx4 %1, %2\n\ts_mov_b32 m0, %0"
               : "=&s"(keep) : "v"(voff), "s"(sbase), "s"(lds_addr) : "memory");
}

struct Ctx {
  __amdgpu_buffer_rsrc_t rs;
  unsigned tag0, tag1;
  int lane, wave, gi, gj;
};

DI u32x4 ld128(const Ctx& x, int voff, int soff) { return __builtin_amdgcn_raw_buffer_load_b128(x.rs, voff, soff, AUX_SC1); }
DI void st128(const Ctx& x, u32x4 v, int voff, int soff) { __builtin_amdgcn_raw_buffer_store_b128(v, x.rs, voff + soff, 0, AUX_SC1); }
DI void st64(const Ctx& x, u32x2 v, int voff, int soff) { __builtin_amdgcn_raw_buffer_store_b64(v, x.rs, voff, soff, AUX_SC1); }

DI void signal(const Ctx& x, int lane, int word) {
  int so = WS_FLAGS + word * FSTR;
  u32x2 tg = {x.tag0, x.tag1};
  int vo = lane == 0 ? 0 : 0x7ffffff0;
  OPAQUE_S(so); OPAQUE_V(tg); OPAQUE_V(vo);
  DRAIN();
  st64(x, tg, vo, so);
}
constexpr int PRE_X = 14, PRE_XB = 5, PRE_OA = 14, PRE_OB = 14, PRE_KV = 8;
template <int PRE>
DI void poll(const Ctx& x, int word) {
  if (PRE) __builtin_amdgcn_s_sleep(PRE);
  for (unsigned spins = 0; spins < SPIN_CAP; ++spins) {
    asm volatile("" ::: "memory");
    const u32x2 v = __builtin_amdgcn_raw_buffer_load_b64(x.rs, word * FSTR, WS_FLAGS, AUX_SC1);
    if (__all(v.x == x.tag0 && v.y == x.tag1)) break;
  }
  __builtin_amdgcn_fence(__ATOMIC_ACQUIRE, "wavefront");
  asm volatile("" ::: "memory");
}

DI u32x4 ld_stats(const Ctx& x, int voff, int soff) {
  const u32x2 a = __builtin_amdgcn_raw_buffer_load_b64(x.rs, voff, soff, AUX_SC1), b = __builtin_amdgcn_raw_buffer_load_b64(x.rs, voff + 128, soff, AUX_SC1);
  return u32x4{a.x, a.y, b.x, b.y};
}
DI void finish_stats(u32x4 p, float& mean, float& rstd) {
  float s1 = __uint_as_float(p.x) + __uint_as_float(p.z), s2 = __uint_as_float(p.y) + __uint_as_float(p.w);
  xsum2(s1, s2);
  mean = s1 * (1.f / 512.f);
  float var = fmaxf(s2 * (1.f / 512.f) - mean * mean, 0.f);
  rstd = __builtin_amdgcn_rsqf(var + 1e-5f);
}
DI u32x2 ln_pack(f32x4 a, f32x4 cs, float mean, float rstd) {
  float v0 = fmaf(-mean, cs.x, a.x) * rstd, v1 = fmaf(-mean, cs.y, a.y) * rstd;
  float v2 = fmaf(-mean, cs.z, a.z) * rstd, v3 = fmaf(-mean, cs.w, a.w) * rstd;
  return u32x2{pk2(v0, v1), pk2(v2, v3)};
}

template <class F>
DI void feature_map(u32x4 in, const u32x4 (&wp)[4], unsigned (&o)[8], F&& filler) {
  f32x4 p[4];
  const f32x4 z = {0.f, 0.f, 0.f, 0.f};
#pragma unroll
  for (int t = 0; t < 4; ++t) p[t] = mfma32(wp[t], in, z);
  filler();
  SB();
  const float m = max3(max3(max3(p[0].x, p[0].y, p[0].z), max3(p[0].w, p[1].x, p[1].y), max3(p[1].z, p[1].w, p[2].x)),
                       max3(p[2].y, p[2].z, p[2].w), max3(max3(p[3].x, p[3].y, p[3].z), p[3].w, p[0].x));
  const float LOG2E = 1.4426950408889634f;
#pragma unroll
  for (int t = 0; t < 4; ++t) p[t] = (p[t] - m) * LOG2E;
#pragma unroll
  for (int t = 0; t < 4; ++t)
#pragma unroll
    for (int r = 0; r < 4; ++r) p[t][r] = __builtin_amdgcn_exp2f(p[t][r]);
  const f32x4 s4 = (p[0] + p[1]) + (p[2] + p[3]);
  const float rs = __builtin_amdgcn_rcpf((s4.x + s4.y) + (s4.z + s4.w));
#pragma unroll
  for (int t = 0; t < 4; ++t) { const f32x4 v = p[t] * rs; o[2 * t] = pk2(v.x, v.y); o[2 * t + 1] = pk2(v.z, v.w); }
}

constexpr int XWIN = 6, OWIN = 8;
constexpr int START_SLEEP = 4, DMA_SLEEP = 1;

struct Pre {
  f32x4 cs[6];
  f32x4 bo, bp;
  int a0, a1, a2;
  int ov;
};
template <int L>
DI void make_pre(Pre& pre, int lane, int w, int gi, int gj) {
  constexpr bool IS_B = L & 1;
  constexpr int LDS_S = (IS_B ? LDS_X : LDS_Y) + 4096 + 64;
  const int c = lane & 15, g = lane >> 4, q = c >> 2, p = c & 3;
  pre.ov = WS_OF + L * FRAG_BUF + (8 * gi + w) * 16384 + gj * 1024 + lane * 16;
  if (IS_B) {
    const float NEG = -1e30f;
    const bool noprev = (w < 2) && gi == 0;
#pragma unroll
    for (int r = 0; r < 4; ++r) {
      const int key = 4 * g + r;
      pre.bo[r] = (c - key >= 0 && c - key <= 7) ? 0.f : NEG;
      pre.bp[r] = (key >= c + 9 && !noprev) ? 0.f : NEG;
    }
    OPAQUE_V(pre.bo); OPAQUE_V(pre.bp);
    pre.a0 = LDS_S + 2048 + w * 1024 + lane * 8;
    pre.a1 = LDS_S + 10240 + (2 + w) * 1024 + c * 32 + g * 8;
    pre.a2 = LDS_S + 10240 + w * 1024 + (4 * g + q) * 32 + p * 8;
  } else {
    pre.a0 = LDS_S + g * 4096 + (w * 16 + c) * 32;
    pre.a1 = LDS_S + 16384 + (w * 16 + c) * 32 + g * 8;
    pre.a2 = LDS_S + (4 * g + q) * 32 + p * 8;
  }
  OPAQUE_V(pre.a0); OPAQUE_V(pre.a1); OPAQUE_V(pre.a2); OPAQUE_V(pre.ov);
}
#define WFRAG(s, ct) (*(LAS u32x4*)((ct) < 2 ? lds_at(wq) + (s) * 2048 + (ct) * 1024 : lds_at(wa) + (s) * 4096 + ((ct) - 2) * 1024))
template <int L>
DI void gemm_phase(const Ctx& x, f32x2& cskeep, u32x4 (&poB)[8], u32x4 (&xf)[16], f32x4 (&acc)[6], float& mean, float& rstd, u32x4& hsp, Pre& pre) {
  constexpr bool IS_B = L & 1;
  constexpr int KVW = IS_B ? LDS_Y : LDS_X, SCR = IS_B ? LDS_X : LDS_Y;
  constexpr int LDS_HF = SCR + 4096, LDS_S = SCR + 4096 + 64;
  int lane = x.lane, w = x.wave, gi = x.gi, gj = x.gj;
  OPAQUE_V(lane); OPAQUE_S(w); OPAQUE_S(gi); OPAQUE_S(gj);
  const int c = lane & 15, g = lane >> 4;
  const int LDS_CS = L < 2 ? SCR + 61440 : SCR + w * 512;
  const int tau = 8 * gi + w;
  const bool has_halo = IS_B && gi > 0;
  constexpr int KIM = LDS_S + 2048, VIMB = KIM + 8192, HP = VIMB + 10240;
  const int wa = KVW + lane * 16, wq = LDS_Z + lane * 16;
  constexpr int C0 = IS_B ? 0 : 2;
  if (L != 1) {
    *(LAS float*)lds_at(LDS_CS + lane * 4) = cskeep.x;
    *(LAS float*)lds_at(LDS_CS + 256 + lane * 4) = cskeep.y;
  } else {
    for (unsigned spins = 0; spins < SPIN_CAP; ++spins) {
      const u32x2 v = *(volatile LAS u32x2*)lds_at(SCR + 61952 + (lane % 3) * 8);
      if (__all(v.x == x.tag0 && v.y == x.tag1)) break;
    }
    cskeep = f32x2{*(volatile LAS float*)lds_at(LDS_CS + lane * 4), *(volatile LAS float*)lds_at(LDS_CS + 256 + (lane & 31) * 4)};
  }
  int fo = ((EDGE_X + L) * 128 + tau) * 16 + (lane & 7);
  if (has_halo && lane >= 8 && lane < 24) fo = ((EDGE_X + L) * 128 + 8 * gi - 2 + ((lane >> 3) & 1)) * 16 + (w < 2 ? (lane & 7) : w);
  constexpr int XW = XWIN, XB = L;
  int xbase = WS_XF + XB * FRAG_BUF + tau * 16384, hbase = WS_XF + L * FRAG_BUF + w * 2048, sbase = WS_XS + XB * XS_BUF + tau * 1024;
  const int hc = 8 + (c & 7);
  int xv = lane * 16, sv = g * 256 + c * 8;
  int hxv = has_halo ? (8 * gi - 2 + (c >> 3)) * 16384 + (g * 16 + hc) * 16 : 0x7ffffff0;
  int hsv = has_halo ? (8 * gi - 2 + w) * 1024 + g * 256 + hc * 8 : 0x7fffff00;
  u32x4 wf[2][6];
#pragma unroll
  for (int ct = C0; ct < 6; ++ct) wf[0][ct] = WFRAG(0, ct);
  OPAQUE_S(xbase); OPAQUE_S(hbase); OPAQUE_S(sbase); OPAQUE_V(xv); OPAQUE_V(sv);
  if (IS_B) { OPAQUE_V(hxv); OPAQUE_V(hsv); }
  make_pre<L>(pre, lane, w, gi, gj);
  poll<(IS_B ? PRE_XB : PRE_X)>(x, fo);
#define LDX(s) ld128(x, xv + ((s) & 3) * 1024, xbase + ((s) >> 2) * 4096)
  u32x4 hx[2], sp;
#pragma unroll
  for (int s = 0; s < XW; ++s) {
    xf[s] = LDX(s);
    if (IS_B && s == 2) {
#pragma unroll
      for (int i = 0; i < 2; ++i) hx[i] = ld128(x, hxv + i * 1024, hbase);
    }
    SB();
  }
  u32x4 hf[4];
  f32x4 hacc[4];
#pragma unroll
  for (int ct = 0; ct < 6; ++ct) acc[ct] = f32x4{0.f, 0.f, 0.f, 0.f};
  SB();
#pragma unroll
  for (int s = 0; s < 16; ++s) {
    if (s + XW < 16) xf[s + XW] = LDX(s + XW);
    if (s == 4) sp = ld_stats(x, sv, sbase);
    if (IS_B && s == 10) hsp = ld_stats(x, w < 2 ? hsv : 0x7fffff00, WS_XS + L * XS_BUF);
    if (s + 1 < 16) {
#pragma unroll
      for (int ct = C0; ct < 6; ++ct) wf[(s + 1) & 1][ct] = WFRAG(s + 1, ct);
    }
#pragma unroll
    for (int ct = C0; ct < 6; ++ct) acc[ct] = mfma32(wf[s & 1][ct], xf[s], acc[ct]);
    if (IS_B) {
      if (s == 3 || s == 4) {
#pragma unroll
        for (int i = 0; i < 4; ++i) hacc[i] = mfma32(hf[i], hx[s - 3], s == 3 ? f32x4{0.f, 0.f, 0.f, 0.f} : hacc[i]);
      }
      if (s == 2 || s == 3) {
#pragma unroll
        for (int i = 0; i < 4; ++i) hf[i] = *(LAS u32x4*)(lds_at(wa + w * 8192) + (s - 2) * 4096 + i * 1024);
      }
      if (s == 5) {
#pragma unroll
        for (int i = 0; i < 4; ++i) *(LAS f32x4*)(lds_at(HP + w * 4096 + lane * 16) + i * 1024) = hacc[i];
      }
      if (s == 7) *(volatile LAS u32x2*)lds_at(LDS_HF + w * 8) = u32x2{x.tag0, x.tag1 + L};
    }
    if (s == 15) {
#pragma unroll
      for (int ct = C0; ct < 6; ++ct) pre.cs[ct] = *(LAS f32x4*)(lds_at(LDS_CS + g * 16) + ct * 64);
    }
    if (s == 10) { OPAQUE_V(sp); finish_stats(sp, mean, rstd); }
    {
#pragma unroll
      for (int i = C0; i < 6; ++i) {
        __builtin_amdgcn_sched_group_barrier(0x008, 1, 0);
        __builtin_amdgcn_sched_group_barrier(0x100, 1, 0);
        if (s == 10) __builtin_amdgcn_sched_group_barrier(0x002, 5, 0);
        if (IS_B && s == 5) __builtin_amdgcn_sched_group_barrier(0x200, 1, 0);
      }
      if (IS_B && s >= 2 && s <= 4) {
#pragma unroll
        for (int i = 0; i < 4; ++i) {
          __builtin_amdgcn_sched_group_barrier(0x008, 1, 0);
          __builtin_amdgcn_sched_group_barrier(0x100, 1, 0);
        }
      }
    }
    SB();
  }
#undef LDX
  if (L == 1) {
    if (w < 4) {
#pragma unroll
      for (int n = 0; n < 8; ++n) poB[n] = ld128(x, lane * 16 + (n & 3) * 1024, WS_WOT + (16 + gj) * 65536 + w * 8192 + (n >> 2) * 4096);
    }
  }
}

template <int L, class F>
DI void mix_phase(const Ctx& x, const u32x4 (&wp)[4], u32x4 (&po)[8], const u32x4 (&xf)[16], f32x4 (&acc)[6], float mean, float rstd, u32x4 hsp,
                  const Pre& pre, F&& late_stores) {
  constexpr bool IS_B = L & 1;
  constexpr int KVW = IS_B ? LDS_Y : LDS_X, SCR = IS_B ? LDS_X : LDS_Y;
  constexpr int LDS_HF = SCR + 4096, LDS_S = SCR + 4096 + 64;
  int lane = x.lane, w = x.wave, gi = x.gi, gj = x.gj;
  OPAQUE_V(lane); OPAQUE_S(w); OPAQUE_S(gi); OPAQUE_S(gj);
  const int c = lane & 15, g = lane >> 4;
  const int LDS_CS = L < 2 ? SCR + 61440 : SCR + w * 512;
  const int tau = 8 * gi + w;
  const bool has_halo = IS_B && gi > 0;
  constexpr int KIM = LDS_S + 2048, VIMB = KIM + 8192, HP = VIMB + 10240;
  const int wa = KVW + lane * 16, wq = LDS_Z + lane * 16;
  constexpr int C0 = IS_B ? 0 : 2;
  const int q = c >> 2, p = c & 3;
  const u32x4 ones = {0x3f803f80u, 0x3f803f80u, 0x3f803f80u, 0x3f803f80u};
  const f32x4 (&cs)[6] = pre.cs;
#define STAGE_WO() do { _Pragma("unroll") for (int n = 0; n < 8; ++n) *(LAS u32x4*)(lds_at(KVW + w * 8192 + lane * 16) + n * 1024) = po[n]; } while (0)
  if (!IS_B) {
    constexpr int PHI = LDS_S, VIM = PHI + 16384, KVOP = VIM + 8192, ZOP = KVOP + 2048;
    {
      u32x2 kn[2];
#pragma unroll
      for (int h = 0; h < 2; ++h) kn[h] = ln_pack(acc[2 + h], cs[2 + h], mean, rstd);
      unsigned ph[8];
      feature_map(u32x4{kn[0].x, kn[0].y, kn[1].x, kn[1].y}, wp, ph, [&]() {
#pragma unroll
        for (int h = 0; h < 2; ++h) *(LAS u32x2*)(lds_at(pre.a1) + h * 4096) = ln_pack(acc[4 + h], cs[4 + h], mean, rstd);
      });
      *(LAS u32x4*)lds_at(pre.a0) = u32x4{ph[0], ph[1], ph[2], ph[3]};
      *(LAS u32x4*)(lds_at(pre.a0) + 16) = u32x4{ph[4], ph[5], ph[6], ph[7]};
    }
    constexpr int e = L >> 1;
    int kvo = WS_KV + e * KV_BUF + ((gj * 16 + gi) * 4 + w) * 1024 + lane * 16;
    int kzo = c == 0 ? WS_KZ + e * KZ_BUF + (gj * 4 + w) * 1024 + gi * 64 + g * 16 : 0x7ffffff0;
    int pa = pre.a2 + w * 4096, pb = pre.a2 + 16384 + (w >> 1) * 4096;
    u32x4 one4 = ones;
    OPAQUE_V(kvo); OPAQUE_V(kzo); OPAQUE_V(pa); OPAQUE_V(pb); OPAQUE_V(one4);
    if (w < 4) __builtin_amdgcn_s_setprio(2);
    BAR();
    if (w < 4) {
      f32x4 kv0 = {0.f, 0.f, 0.f, 0.f}, z0 = kv0;
      u32x4 a[4], b[4];
#pragma unroll
      for (int u = 0; u < 4; ++u) { a[u] = tr_frag(lds_at(pa) + u * 1024, 512); b[u] = tr_frag(lds_at(pb) + u * 1024, 512); }
      SB();
#pragma unroll
      for (int u = 0; u < 4; ++u) { kv0 = mfma32(a[u], b[u], kv0); z0 = mfma32(a[u], one4, z0); }
      st128(x, __builtin_bit_cast(u32x4, kv0), kvo, 0);
      st128(x, __builtin_bit_cast(u32x4, z0), kzo, 0);
      __builtin_amdgcn_s_setprio(0);
      if (L > 0) STAGE_WO();
      signal(x, lane, (e * 64 + gj * 4 + w) * 16 + gi);
      if (L == 0) late_stores();
    } else {
      __builtin_amdgcn_s_setprio(1);
    }
    if (L == 0) {
#pragma unroll
      for (int n = 0; n < 8; ++n) po[n] = ld128(x, lane * 16 + (n & 3) * 1024, WS_WOT + gj * 65536 + w * 8192 + (n >> 2) * 4096);
      SB();
    }
    u32x4 pq[2];
    {
      constexpr int QR = 6;
      u32x4 f[QR];
      f32x4 csq[2];
#pragma unroll
      for (int i = 0; i < QR; ++i) f[i] = WFRAG(i >> 1, i & 1);
#pragma unroll
      for (int i = 0; i < 32; ++i) {
        acc[i & 1] = mfma32(f[i % QR], xf[i >> 1], acc[i & 1]);
        if (i + QR < 32) f[i % QR] = WFRAG((i + QR) >> 1, (i + QR) & 1);
        else if (i + QR < 34) csq[i + QR - 32] = *(LAS f32x4*)(lds_at(LDS_CS + g * 16) + (i + QR - 32) * 64);
        SB();
      }
      u32x2 qn[2];
#pragma unroll
      for (int h = 0; h < 2; ++h) qn[h] = ln_pack(acc[h], csq[h], mean, rstd);
      unsigned ph[8];
      feature_map(u32x4{qn[0].x, qn[0].y, qn[1].x, qn[1].y}, wp, ph, []() {});
#pragma unroll
      for (int r = 0; r < 2; ++r) { swap16(ph[r], ph[2 + r]); swap16(ph[4 + r], ph[6 + r]); }
#pragma unroll
      for (int r = 0; r < 2; ++r) { swap32(ph[r], ph[4 + r]); swap32(ph[2 + r], ph[6 + r]); }
      pq[0] = u32x4{ph[0], ph[1], ph[2], ph[3]};
      pq[1] = u32x4{ph[4], ph[5], ph[6], ph[7]};
      OPAQUE_V(pq[0]); OPAQUE_V(pq[1]);
    }
    if (w >= 4) {
      const int wi = w - 4;
      int lv = lane * 16;
      int sbase = WS_KV + e * KV_BUF + (gj * 64 + wi) * 1024;
      int wr = KVOP + (wi >> 1) * 1024 + lane * 16 + (wi & 1) * 8;
      OPAQUE_S(sbase); OPAQUE_V(lv); OPAQUE_V(wr);
      poll<PRE_KV>(x, (e * 64 + gj * 4 + wi) * 16 + c);
      const u32x4 zu = ld128(x, lv, WS_KZ + e * KZ_BUF + (gj * 4 + wi) * 1024);
      u32x4 a[16];
#pragma unroll
      for (int i = 0; i < 16; ++i) { a[i] = ld128(x, lv, sbase + i * 4096); SB(); }
      STAGE_WO();
      const f32x4 zs = sum16q(zu);
      if (lane < 4) *(LAS u32x2*)lds_at(ZOP + (wi >> 1) * 64 + lane * 16 + (wi & 1) * 8) = u32x2{pk2(zs.x, zs.y), pk2(zs.z, zs.w)};
      f32x4 t[4];
#pragma unroll
      for (int i = 0; i < 4; ++i)
        t[i] = (__builtin_bit_cast(f32x4, a[4 * i]) + __builtin_bit_cast(f32x4, a[4 * i + 1])) + (__builtin_bit_cast(f32x4, a[4 * i + 2]) + __builtin_bit_cast(f32x4, a[4 * i + 3]));
      const f32x4 s0 = (t[0] + t[1]) + (t[2] + t[3]);
      *(LAS u32x2*)lds_at(wr) = u32x2{pk2(s0.x, s0.y), pk2(s0.z, s0.w)};
      __builtin_amdgcn_s_setprio(0);
    } else if (L == 0) {
      if (w < 3) signal(x, lane, (EDGE_SETUP * 128 + 64 + gj * 4 + w) * 16 + gi);
      STAGE_WO();
    }
    int za = ZOP + g * 16, ka = KVOP + lane * 16;
    OPAQUE_V(za); OPAQUE_V(ka);
    BAR();
    {
      const f32x4 z = {0.f, 0.f, 0.f, 0.f};
      u32x4 kvop[2], zop[2];
#pragma unroll
      for (int h = 0; h < 2; ++h) zop[h] = *(LAS u32x4*)(lds_at(za) + h * 64);
#pragma unroll
      for (int h = 0; h < 2; ++h) kvop[h] = *(LAS u32x4*)(lds_at(ka) + h * 1024);
      SB();
      f32x4 num[2], den[2];
#pragma unroll
      for (int h = 0; h < 2; ++h) den[h] = mfma32(zop[h], pq[h], z);
#pragma unroll
      for (int h = 0; h < 2; ++h) num[h] = mfma32(kvop[h], pq[h], z);
      SB();
      unsigned o[4];
#pragma unroll
      for (int h = 0; h < 2; ++h) {
        const float r = __builtin_amdgcn_rcpf(den[h].x + 1e-6f);
        o[2 * h] = pk2(num[h].x * r, num[h].y * r); o[2 * h + 1] = pk2(num[h].z * r, num[h].w * r);
      }
      st128(x, u32x4{o[0], o[1], o[2], o[3]}, pre.ov, 0);
    }
  } else {
    constexpr int VIM = VIMB;
    u32x2 qn[2], kn[2], kp[2];
#pragma unroll
    for (int h = 0; h < 2; ++h) {
      kn[h] = ln_pack(acc[2 + h], cs[2 + h], mean, rstd);
      *(LAS u32x2*)(lds_at(pre.a0) + h * 512) = kn[h];
      *(LAS u32x2*)(lds_at(pre.a1) + h * 512) = ln_pack(acc[4 + h], cs[4 + h], mean, rstd);
    }
    if (w < 2) {
      for (unsigned spins = 0; spins < SPIN_CAP; ++spins) {
        const u32x2 v = *(volatile LAS u32x2*)lds_at(LDS_HF + (lane & 7) * 8);
        if (__all(v.x == x.tag0 && v.y == x.tag1 + L)) break;
      }
      float hm, hr;
      finish_stats(hsp, hm, hr);
      const int src = HP + (c < 8 ? 2048 : 0) + (g * 16 + w * 8 + (c & 7)) * 16;
#pragma unroll
      for (int h = 0; h < 2; ++h) {
        f32x4 a[8];
#pragma unroll
        for (int i = 0; i < 8; ++i) a[i] = *(LAS f32x4*)(lds_at(src) + i * 4096 + h * 1024);
        const f32x4 sum = ((a[0] + a[1]) + (a[2] + a[3])) + ((a[4] + a[5]) + (a[6] + a[7]));
        const f32x4 csw = c < 8 ? cs[4 + h] : cs[2 + h];
        u32x2 v = ln_pack(sum, csw, hm, hr);
        if (!has_halo) v = u32x2{0u, 0u};
        kp[h] = c < 8 ? u32x2{0u, 0u} : v;
        *(LAS u32x2*)lds_at(VIM + (w * 2 + h) * 512 + (c ^ 8) * 32 + g * 8) = c < 8 ? v : u32x2{0u, 0u};
      }
    }
#pragma unroll
    for (int h = 0; h < 2; ++h) qn[h] = ln_pack(acc[h], cs[h], mean, rstd);
    f32x4 so[2];
#pragma unroll
    for (int h = 0; h < 2; ++h) so[h] = mfma16(kn[h], qn[h], pre.bo);
    if (w < 2) {
#pragma unroll
      for (int h = 0; h < 2; ++h) *(LAS u32x2*)(lds_at(pre.a0 - 2048) + h * 512) = kp[h];
    }
    SB();
    BAR();
    {
      const f32x4 z = {0.f, 0.f, 0.f, 0.f};
      const float SC = 0.25f * 1.4426950408889634f;
      f32x4 sp_[2];
      u32x2 vo[2], vp[2], kq[2];
      float m[2];
#pragma unroll
      for (int h = 0; h < 2; ++h) kq[h] = *(LAS u32x2*)(lds_at(pre.a0 - 2048) + h * 512);
#pragma unroll
      for (int h = 0; h < 2; ++h) {
        vp[h] = tr_read(lds_at(pre.a2) + h * 512);
        vo[h] = tr_read(lds_at(pre.a2) + 2048 + h * 512);
      }
      SB();
#pragma unroll
      for (int h = 0; h < 2; ++h) sp_[h] = mfma16(kq[h], qn[h], pre.bp);
#pragma unroll
      for (int h = 0; h < 2; ++h)
        m[h] = max3(max3(so[h].x, so[h].y, so[h].z), max3(so[h].w, sp_[h].x, sp_[h].y), max3(sp_[h].z, sp_[h].w, so[h].x));
      swap32f(m[0], m[1]);
      float t = max2(m[0], m[1]), u = t;
      swap16f(t, u);
      float a = max2(t, u), b = a;
      swap32f(a, b);
      m[0] = a; m[1] = b;
      u32x4 P[2];
#pragma unroll
      for (int h = 0; h < 2; ++h) {
        const f32x4 d0 = (so[h] - m[h]) * SC, d1 = (sp_[h] - m[h]) * SC;
        float e[8];
#pragma unroll
        for (int r = 0; r < 4; ++r) { e[r] = __builtin_amdgcn_exp2f(d0[r]); e[4 + r] = __builtin_amdgcn_exp2f(d1[r]); }
        P[h] = u32x4{pk2(e[0], e[1]), pk2(e[2], e[3]), pk2(e[4], e[5]), pk2(e[6], e[7])};
      }
      SB();
      f32x4 num[2], den[2];
#pragma unroll
      for (int h = 0; h < 2; ++h) den[h] = mfma32(ones, P[h], z);
#pragma unroll
      for (int h = 0; h < 2; ++h) num[h] = mfma32(u32x4{vo[h].x, vo[h].y, vp[h].x, vp[h].y}, P[h], z);
      SB();
      unsigned o[4];
#pragma unroll
      for (int h = 0; h < 2; ++h) {
        const float r = __builtin_amdgcn_rcpf(den[h].x);
        o[2 * h] = pk2(num[h].x * r, num[h].y * r); o[2 * h + 1] = pk2(num[h].z * r, num[h].w * r);
      }
      st128(x, u32x4{o[0], o[1], o[2], o[3]}, pre.ov, 0);
    }
    SB();
    STAGE_WO();
  }
  signal(x, lane, ((EDGE_O + L) * 128 + tau) * 16 + gj);
  BAR();
#undef WFRAG
#undef STAGE_WO
}

template <int L>
DI void oproj_phase(const Ctx& x, f32x4 (&res)[4], u32x4 (&poB)[8], u16* out) {
  constexpr bool IS_B = L & 1;
  constexpr int KVW = IS_B ? LDS_Y : LDS_X, SCR = IS_B ? LDS_X : LDS_Y;
  int lane = x.lane, w = x.wave, gi = x.gi, gj = x.gj;
  OPAQUE_V(lane); OPAQUE_S(w); OPAQUE_S(gi); OPAQUE_S(gj);
  const int c = lane & 15, g = lane >> 4;
  const int tau = 8 * gi + 4 * (gj >> 3) + w;
  f32x4 cb[2] = {f32x4{0.f, 0.f, 0.f, 0.f}, f32x4{0.f, 0.f, 0.f, 0.f}};
  if (w < 4) {
    const int wb = KVW + lane * 16;
    constexpr int PD = 3, OW = OWIN;
    u32x4 wf[PD][4];
#pragma unroll
    for (int s = 0; s < PD; ++s)
#pragma unroll
      for (int ct = 0; ct < 4; ++ct) wf[s][ct] = *(LAS u32x4*)(lds_at(wb) + s * 4096 + ct * 1024);
    int xo = WS_XF + (L + 1) * FRAG_BUF + (tau * 16 + (gj & 7) * 2) * 1024 + lane * 16;
    int so = g == (c >> 2) ? WS_XS + (L + 1) * XS_BUF + tau * 1024 + (gj & 7) * 128 + c * 8 : 0x7ffffff0;
    u32x4 one4 = {0x3f803f80u, 0x3f803f80u, 0x3f803f80u, 0x3f803f80u};
    int obase = WS_OF + L * FRAG_BUF + tau * 16384, lv = lane * 16;
    if (L < 3) { OPAQUE_V(xo); OPAQUE_V(so); OPAQUE_V(one4); }
    OPAQUE_S(obase); OPAQUE_V(lv);
    poll<(IS_B ? PRE_OB : PRE_OA)>(x, ((EDGE_O + L) * 128 + tau) * 16 + c);
    u32x4 of[16];
#pragma unroll
    for (int s = 0; s < OW; ++s) { of[s] = ld128(x, lv + (s & 3) * 1024, obase + (s >> 2) * 4096); SB(); }
#pragma unroll
    for (int s = 0; s < 16; ++s) {
      if (s + OW < 16) of[s + OW] = ld128(x, lv + ((s + OW) & 3) * 1024, obase + ((s + OW) >> 2) * 4096);
#pragma unroll
      for (int ct = 0; ct < 4; ++ct) {
        res[ct] = mfma32(wf[s % PD][ct], of[s], res[ct]);
        if (s + PD < 16) wf[s % PD][ct] = *(LAS u32x4*)(lds_at(wb) + (s + PD) * 4096 + ct * 1024);
      }
      SB();
    }
    const int nb = gj & 7;
    u32x4 f[2];
#pragma unroll
    for (int a = 0; a < 2; ++a)
      f[a] = u32x4{pk2(res[2 * a].x, res[2 * a].y), pk2(res[2 * a].z, res[2 * a].w), pk2(res[2 * a + 1].x, res[2 * a + 1].y), pk2(res[2 * a + 1].z, res[2 * a + 1].w)};
    if (L < 3) {
      const f32x4 z = {0.f, 0.f, 0.f, 0.f};
      const f32x4 sq = mfma32(f[1], f[1], mfma32(f[0], f[0], z)), sm = mfma32(one4, f[1], mfma32(one4, f[0], z));
#pragma unroll
      for (int a = 0; a < 2; ++a) st128(x, f[a], xo + a * 1024, 0);
      st64(x, u32x2{__float_as_uint(sm.x), __float_as_uint(sel4(sq, c & 3))}, so, 0);
      signal(x, lane, ((EDGE_X + L + 1) * 128 + tau) * 16 + nb);
    } else {
      const int tok = 32 * (tau >> 1) + 2 * c + (tau & 1);
#pragma unroll
      for (int a = 0; a < 2; ++a) {
        *(GAS u32x4*)(out + tok * 512 + nb * 64 + a * 32 + 8 * g) = f[a];
      }
    }
  } else if (L < 3) {
    if (!IS_B && L == 0) {
      poll<0>(x, (EDGE_SETUP * 128 + 64 + gj * 4) * 16 + (lane < 48 ? lane : 0));
#pragma unroll
      for (int n = 0; n < 2; ++n) cb[n] = sum16q(ld128(x, lane * 16, WS_CSP + (16 + gj) * 6144 + ((w < 7 ? w - 4 : 0) * 2 + n) * 1024));
    }
    const int src = WS_WT + ((IS_B ? 0 : 16) + gj) * 98304;
    const int wi = w - 4;
    u32x4 f[24];
#define FSRC(n) ((n) < 16 ? src + wi * 16384 + ((n) >> 2) * 4096 : src + 65536 + wi * 8192 + (((n) - 16) >> 2) * 4096)
#define FDST(n) ((n) < 16 ? lds_at(SCR + wi * 16384 + lane * 16) + (n) * 1024 : lds_at(LDS_Z + wi * 8192 + lane * 16) + ((n) - 16) * 1024)
#pragma unroll
    for (int n = 0; n < 24; ++n) {
      f[n] = ld128(x, lane * 16 + (n & 3) * 1024, FSRC(n));
      __builtin_amdgcn_s_sleep(DMA_SLEEP);
      if (n >= 6) *(LAS u32x4*)FDST(n - 6) = f[n - 6];
      SB();
    }
#pragma unroll
    for (int n = 18; n < 24; ++n) *(LAS u32x4*)FDST(n) = f[n];
#undef FSRC
#undef FDST
  }
  if (!IS_B && L == 0) {
    if (w >= 4) {
#pragma unroll
      for (int n = 0; n < 8; ++n) poB[n] = ld128(x, lane * 16 + (n & 3) * 1024, WS_WOT + (16 + gj) * 65536 + w * 8192 + (n >> 2) * 4096);
    }
  }
  if (L < 3) BAR();
  if (L == 0 && w >= 4 && w < 7) {
    if (lane < 4) {
#pragma unroll
      for (int n = 0; n < 2; ++n) *(LAS f32x4*)lds_at(KVW + 61440 + ((w - 4) * 2 + n) * 64 + lane * 16) = cb[n];
    }
    *(volatile LAS u32x2*)lds_at(KVW + 61952 + (w - 4) * 8) = u32x2{x.tag0, x.tag1};
  }
}

__global__ void __launch_bounds__(512, 2)
fused_fwd(const u16* __restrict__ x0, const u16* __restrict__ wA, const u16* __restrict__ wphi, const u16* __restrict__ woA,
          const u16* __restrict__ wB, const u16* __restrict__ woB, u16* __restrict__ out, unsigned char* __restrict__ wsp) {
  extern __shared__ __attribute__((aligned(16))) unsigned char smem[];
  asm volatile("" : "+s"(x0), "+s"(wA), "+s"(wphi), "+s"(woA), "+s"(wB), "+s"(woB), "+s"(out), "+s"(wsp));
  Ctx x;
  x.lane = threadIdx.x & 63;
  x.wave = __builtin_amdgcn_readfirstlane(threadIdx.x >> 6);
  {
    constexpr int NJ = MAP_NJ, NI = 32 / NJ, XJ = 16 / NJ;
    const int xc = blockIdx.x & 7, r = blockIdx.x >> 3;
    x.gi = (xc / XJ) * NI + r / NJ;
    x.gj = (xc % XJ) * NJ + r % NJ;
  }
  x.rs = __builtin_amdgcn_make_buffer_rsrc((void*)wsp, 0, WS_END, 0x00020000);
  const int lane = x.lane, w = x.wave, c = lane & 15, g = lane >> 4, q = c >> 2, p = c & 3, gi = x.gi, gj = x.gj;
  const u32x4 ones = {0x3f803f80u, 0x3f803f80u, 0x3f803f80u, 0x3f803f80u};
  const int tau0 = 8 * gi + w;
  const int xstg = (w < 4 ? LDS_Y : LDS_X) + (w & 3) * 16384;
  f32x4 res[4];
  auto unit_dma = [&](int u, int slot) {
    const bool isw = u < 6;
    const char* src = isw ? (const char*)(u < 3 ? wA : wB) + (size_t)gi * 98304 + (u % 3) * 1024 + gj * 64
                          : (const char*)(u < 8 ? woA : woB) + (size_t)gi * 32768 + (gj & 7) * 128 + (u & 1) * 64;
    const int ld = isw ? 3072 : 1024;
    const unsigned vo = (lane >> 2) * ld + (lane & 3) * 16;
    dma16(src, vo, slot);
    dma16(src + 16 * ld, vo, slot + 1024);
  };
  auto unit_read = [&](int u, int slot, u32x4 (&f)[2]) {
    const bool isw = u < 6;
    const int ad = slot + (isw ? (8 * g + q) * 64 + p * 8 : (4 * g + q) * 64 + p * 16);
    const int sec = isw ? 256 : 1024, astr = isw ? 32 : 8;
#pragma unroll
    for (int a = 0; a < 2; ++a) {
      const u32x2 lo = tr_read(lds_at(ad + a * astr)), hi = tr_read(lds_at(ad + a * astr + sec));
      f[a] = u32x4{lo.x, lo.y, hi.x, hi.y};
    }
  };
  auto unit_store = [&](int u, const u32x4 (&f)[2]) {
    const bool isw = u < 6;
    const int s3 = u % 3;
    const int dst = isw ? WS_WT + ((u / 3) * 16 + gj) * 98304 + (s3 == 0 ? 65536 + gi * 2048 : gi * 4096 + (s3 - 1) * 2048)
                        : WS_WOT + (((u - 6) >> 1) * 16 + gj) * 65536 + gi * 4096 + (u & 1) * 2048;
#pragma unroll
    for (int a = 0; a < 2; ++a) st128(x, f[a], lane * 16 + a * 1024, dst);
    if (isw) {
      const f32x4 s0 = mfma32(f[0], ones, f32x4{0.f, 0.f, 0.f, 0.f}), s1 = mfma32(f[1], ones, f32x4{0.f, 0.f, 0.f, 0.f});
      if (c < 2) st128(x, __builtin_bit_cast(u32x4, c ? s1 : s0), g * 16 + c * 1024, WS_CSP + (((u / 3) * 16 + gj) * 6 + s3 * 2) * 1024 + gi * 64);
    }
  };
  auto load_tag = [&]() {
    unsigned long long t;
    asm volatile("s_load_dwordx2 %0, %1, 0x0\n\ts_waitcnt lgkmcnt(0)" : "=s"(t) : "s"(wsp + WS_EPOCH) : "memory");
    unsigned long long z = t + 0x9E3779B97F4A7C15ull;
    z = (z ^ (z >> 30)) * 0xBF58476D1CE4E5B9ull;
    z = (z ^ (z >> 27)) * 0x94D049BB133111EBull;
    z ^= z >> 31;
    if (z == t) z = ~z;
    x.tag0 = (unsigned)z; x.tag1 = (unsigned)(z >> 32);
  };
  const int zs = LDS_Z + (w & 3) * 8192;
  if (w < 4) {
    const int u0 = w < 3 ? w : 6;
    unit_dma(u0, zs);
    if (w == 3) unit_dma(7, zs + 2048);
    load_tag();
    DRAIN();
    u32x4 f[2];
    unit_read(u0, zs, f);
    unit_store(u0, f);
    if (w == 3) { unit_read(7, zs + 2048, f); unit_store(7, f); }
    signal(x, lane, (EDGE_SETUP * 128 + gj * 4 + w) * 16 + gi);
  } else {
    __builtin_amdgcn_s_sleep(START_SLEEP);
  }
  {
    const char* xrow = (const char*)x0 + (32 * (tau0 >> 1) + (tau0 & 1)) * 1024;
#pragma unroll
    for (int n = 0; n < 16; ++n) dma16(xrow + n * 2048, (lane ^ (n << 2)) << 4, xstg + n * 1024);
  }
  if (w < 4) {
    if (w < 3) unit_dma(3 + w, zs + 2048);
    if (w < 2) unit_dma(8 + w, zs + 4096);
    const int tau = 8 * gi + 4 * (gj >> 3) + w;
    const unsigned rvo = (32 * (tau >> 1) + 2 * (lane >> 3) + (tau & 1)) * 1024 + ((lane & 7) << 4);
    dma16((const char*)x0 + (gj & 7) * 128, rvo, zs + 6144);
    dma16((const char*)x0 + (gj & 7) * 128 + 16384, rvo, zs + 7168);
    dma16((const char*)wphi + gj * 1024, lane * 16, zs);
  }
  u32x4 wp[4];
  auto read_wp = [&]() {
    const unsigned sg = (q & 1) ? 0x80008000u : 0u;
#pragma unroll
    for (int t = 0; t < 4; ++t) {
      const u32x2 v = tr_read(lds_at(zs + (p >> 1) * 512 + (4 * g + q) * 32) + t * 8);
      wp[t] = (q >> 1) ? u32x4{0u, 0u, v.x ^ sg, v.y ^ sg} : u32x4{v.x ^ sg, v.y ^ sg, 0u, 0u};
    }
    OPAQUE_V(wp[0]); OPAQUE_V(wp[1]); OPAQUE_V(wp[2]); OPAQUE_V(wp[3]);
  };
  if (w >= 4) load_tag();
  DRAIN();
  u32x4 xf[16], fd0[2], fd1[2];
#pragma unroll
  for (int s = 0; s < 16; ++s) xf[s] = *(LAS u32x4*)lds_at(xstg + c * 1024 + ((s ^ c) << 6) + g * 16);
  unit_read(3, zs + 2048, fd0);
  unit_read(8, zs + 4096, fd1);
#pragma unroll
  for (int a = 0; a < 2; ++a) {
    const u32x4 v = *(LAS u32x4*)lds_at(zs + 6144 + c * 128 + ((a * 4 + g) << 4));
    res[2 * a] = f32x4{bflo(v.x), bfhi(v.x), bflo(v.y), bfhi(v.y)};
    res[2 * a + 1] = f32x4{bflo(v.z), bfhi(v.z), bflo(v.w), bfhi(v.w)};
  }
  asm volatile("s_waitcnt lgkmcnt(0)" : "+v"(xf[0]), "+v"(xf[1]), "+v"(xf[2]), "+v"(xf[3]), "+v"(xf[4]), "+v"(xf[5]), "+v"(xf[6]), "+v"(xf[7]),
               "+v"(xf[8]), "+v"(xf[9]), "+v"(xf[10]), "+v"(xf[11]), "+v"(xf[12]), "+v"(xf[13]), "+v"(xf[14]), "+v"(xf[15]),
               "+v"(fd0[0]), "+v"(fd0[1]), "+v"(fd1[0]), "+v"(fd1[1]), "+v"(res[0]), "+v"(res[1]), "+v"(res[2]), "+v"(res[3]) :: "memory");
  float mean, rstd;
  auto row_stats = [&]() {
    f32x4 accs = {0.f, 0.f, 0.f, 0.f}, accq = accs;
#pragma unroll
    for (int n = 0; n < 16; ++n) { accs = mfma32(ones, xf[n], accs); accq = mfma32(xf[n], xf[n], accq); }
    const float dq = sel4(accq, p);
    finish_stats(u32x4{__float_as_uint(accs.x * 0.25f), g == q ? __float_as_uint(dq) : 0u, 0u, 0u}, mean, rstd);
    OPAQUE_V(mean); OPAQUE_V(rstd);
  };
  row_stats();
  u32x4 fq[8];
  f32x4 cb[2];
  if (w >= 4) {
    poll<0>(x, (EDGE_SETUP * 128 + gj * 4) * 16 + lane);
    u32x4 f[16], cp[2];
#pragma unroll
    for (int n = 0; n < 16; ++n) { f[n] = ld128(x, lane * 16 + (n & 3) * 1024, WS_WT + gj * 98304 + (w - 4) * 16384 + (n >> 2) * 4096); SB(); }
#pragma unroll
    for (int n = 0; n < 2; ++n) cp[n] = ld128(x, lane * 16, WS_CSP + gj * 6144 + ((w < 7 ? w - 4 : 0) * 2 + n) * 1024);
#pragma unroll
    for (int n = 0; n < 8; ++n) { fq[n] = ld128(x, lane * 16 + (n & 3) * 1024, WS_WT + gj * 98304 + 65536 + (w - 4) * 8192 + (n >> 2) * 4096); SB(); }
#pragma unroll
    for (int n = 0; n < 16; ++n) *(LAS u32x4*)(lds_at(xstg + lane * 16) + n * 1024) = f[n];
#pragma unroll
    for (int n = 0; n < 2; ++n) cb[n] = sum16q(cp[n]);
  } else {
#pragma unroll
    for (int n = 0; n < 8; ++n) fq[n] = u32x4{0u, 0u, 0u, 0u};
    cb[0] = f32x4{0.f, 0.f, 0.f, 0.f}; cb[1] = cb[0];
    read_wp();
  }
  BAR();
  if (w >= 4) {
    read_wp();
    if (w < 7 && lane < 4) {
#pragma unroll
      for (int n = 0; n < 2; ++n) *(LAS f32x4*)lds_at(LDS_Y + 61440 + ((w - 4) * 2 + n) * 64 + lane * 16) = cb[n];
    }
    if (w < 7) *(volatile LAS u32x2*)lds_at(LDS_Y + 61952 + (w - 4) * 8) = u32x2{x.tag0, x.tag1};
#pragma unroll
    for (int n = 0; n < 8; ++n) *(LAS u32x4*)(lds_at(LDS_Z + (w - 4) * 8192 + lane * 16) + n * 1024) = fq[n];
  }
  Pre pre;
  make_pre<0>(pre, lane, w, gi, gj);
  u32x2 rdy;
  u32x4 poA[8], poB[8];
  f32x4 acc[6];
#pragma unroll
  for (int ct = 0; ct < 6; ++ct) acc[ct] = f32x4{0.f, 0.f, 0.f, 0.f};
  {
    u32x4 wf[2][4];
#pragma unroll
    for (int ct = 0; ct < 4; ++ct) wf[0][ct] = *(LAS u32x4*)(lds_at(LDS_X + lane * 16) + ct * 1024);
    SB();
#pragma unroll
    for (int s = 0; s < 16; ++s) {
      if (s + 1 < 16) {
#pragma unroll
        for (int ct = 0; ct < 4; ++ct) wf[(s + 1) & 1][ct] = *(LAS u32x4*)(lds_at(LDS_X + lane * 16) + (s + 1) * 4096 + ct * 1024);
      }
#pragma unroll
      for (int ct = 0; ct < 4; ++ct) acc[2 + ct] = mfma32(wf[s & 1][ct], xf[s], acc[2 + ct]);
      if (s == 12) rdy = *(volatile LAS u32x2*)lds_at(LDS_Y + 61952 + (lane % 3) * 8);
      if (s + 1 < 16) {
#pragma unroll
        for (int i = 0; i < 4; ++i) {
          __builtin_amdgcn_sched_group_barrier(0x008, 1, 0);
          __builtin_amdgcn_sched_group_barrier(0x100, 1, 0);
        }
      }
      SB();
    }
  }
  if (__builtin_expect(!__all(rdy.x == x.tag0 && rdy.y == x.tag1), 0)) {
    for (unsigned spins = 0; spins < SPIN_CAP; ++spins) {
      const u32x2 v = *(volatile LAS u32x2*)lds_at(LDS_Y + 61952 + (lane % 3) * 8);
      if (__all(v.x == x.tag0 && v.y == x.tag1)) break;
    }
  }
  f32x2 csA = {*(volatile LAS float*)lds_at(LDS_Y + 61440 + lane * 4), *(volatile LAS float*)lds_at(LDS_Y + 61440 + 256 + (lane & 31) * 4)};
  f32x2 csB = {0.f, 0.f};
  u32x4 hsp = {0u, 0u, 0u, 0u};
#pragma unroll
  for (int ct = 2; ct < 6; ++ct) pre.cs[ct] = *(volatile LAS f32x4*)(lds_at(LDS_Y + 61440 + g * 16) + ct * 64);
  auto none = []() {};
  mix_phase<0>(x, wp, poA, xf, acc, mean, rstd, hsp, pre, [&]() {
    if (w < 3) unit_store(3 + w, fd0);
    if (w < 2) unit_store(8 + w, fd1);
  });
  oproj_phase<0>(x, res, poB, out);
  if (blockIdx.x == 0 && threadIdx.x == 0) st64(x, u32x2{x.tag0, x.tag1}, 0, WS_EPOCH);
  gemm_phase<1>(x, csB, poB, xf, acc, mean, rstd, hsp, pre);
  mix_phase<1>(x, wp, poB, xf, acc, mean, rstd, hsp, pre, none);
  oproj_phase<1>(x, res, poB, out);
  gemm_phase<2>(x, csA, poB, xf, acc, mean, rstd, hsp, pre);
  mix_phase<2>(x, wp, poA, xf, acc, mean, rstd, hsp, pre, none);
  oproj_phase<2>(x, res, poB, out);
  gemm_phase<3>(x, csB, poB, xf, acc, mean, rstd, hsp, pre);
  mix_phase<3>(x, wp, poB, xf, acc, mean, rstd, hsp, pre, none);
  oproj_phase<3>(x, res, poB, out);
}

extern "C" void kernel_launch(void* const* d_in, const int* in_sizes, int n_in, void* d_out, int out_size, void* d_ws, size_t ws_size,
                              hipStream_t stream) {
  (void)in_sizes; (void)n_in; (void)out_size; (void)ws_size;
  (void)hipFuncSetAttribute((const void*)fused_fwd, hipFuncAttributeMaxDynamicSharedMemorySize, LDS_BYTES);
  fused_fwd<<<256, 512, LDS_BYTES, stream>>>((const u16*)d_in[0], (const u16*)d_in[1], (const u16*)d_in[2], (const u16*)d_in[3],
                                             (const u16*)d_in[5], (const u16*)d_in[6], (u16*)d_out, (unsigned char*)d_ws);
}
```

```cpp
#include <hip/hip_runtime.h>
#include <stdint.h>

typedef unsigned short u16;
typedef __bf16 bf16x8 __attribute__((ext_vector_type(8)));
typedef __bf16 bf16x2 __attribute__((ext_vector_type(2)));
typedef short s16x4 __attribute__((ext_vector_type(4)));
typedef float f32x4 __attribute__((ext_vector_type(4)));
typedef float f32x2 __attribute__((ext_vector_type(2)));
typedef unsigned u32x2 __attribute__((ext_vector_type(2)));
typedef unsigned u32x4 __attribute__((ext_vector_type(4)));
#define LAS __attribute__((address_space(3)))
#define GAS __attribute__((address_space(1)))
#define DI __device__ __forceinline__

constexpr int WS_EPOCH = 0;
constexpr int FSTR = 64;
constexpr int WS_FLAGS = 24 << 20;
constexpr int EDGE_X = 0, EDGE_O = 4, EDGE_SETUP = 8;
constexpr int XS_BUF = 128 * 1024;
constexpr int WS_XS = 262144 - XS_BUF;
constexpr int WS_KZ = WS_XS + 4 * XS_BUF;
constexpr int KZ_BUF = 16 * 4 * 1024;
constexpr int WS_CSP = WS_KZ + 2 * KZ_BUF;
constexpr int FRAG_BUF = 2 << 20;
constexpr int WS_KV = 1 << 20;
constexpr int KV_BUF = 16 * 16 * 4 * 1024;
constexpr int WS_XF = WS_KV + 2 * KV_BUF - FRAG_BUF;
constexpr int WS_OF = WS_XF + 4 * FRAG_BUF;
constexpr int WS_WT = WS_OF + 4 * FRAG_BUF;
constexpr int WS_WOT = WS_WT + 2 * 16 * 98304;
constexpr int WS_END = 28 << 20;
static_assert(WS_WOT + 2 * 16 * 65536 <= WS_FLAGS && WS_FLAGS + 9 * 128 * 16 * FSTR <= WS_END && WS_CSP + 2 * 16 * 6144 <= WS_KV, "workspace map");

constexpr int LDS_X = 0, LDS_Y = 65536, LDS_Z = 131072;
constexpr int LDS_BYTES = 163840;
constexpr int AUX_SC1 = 16;
constexpr int MAP_NJ = 8;
constexpr unsigned SPIN_CAP = 1u << 16;

DI unsigned pk2(float a, float b) { f32x2 v = {a, b}; return __builtin_bit_cast(unsigned, __builtin_convertvector(v, bf16x2)); }
DI float bflo(unsigned u) { return __uint_as_float(u << 16); }
DI float bfhi(unsigned u) { return __uint_as_float(u & 0xffff0000u); }
DI f32x4 mfma32(u32x4 a, u32x4 b, f32x4 c) {
  return __builtin_amdgcn_mfma_f32_16x16x32_bf16(__builtin_bit_cast(bf16x8, a), __builtin_bit_cast(bf16x8, b), c, 0, 0, 0);
}
DI f32x4 mfma16(u32x2 a, u32x2 b, f32x4 c) {
  return __builtin_amdgcn_mfma_f32_16x16x16bf16_1k(__builtin_bit_cast(s16x4, a), __builtin_bit_cast(s16x4, b), c, 0, 0, 0);
}
DI LAS unsigned char* lds_at(int off) { return (LAS unsigned char*)(uintptr_t)(unsigned)off; }
DI u32x2 tr_read(LAS unsigned char* p) { return __builtin_bit_cast(u32x2, __builtin_amdgcn_ds_read_tr16_b64_v4i16((LAS s16x4*)p)); }
DI u32x4 tr_frag(LAS unsigned char* p, int second) {
  u32x2 a = tr_read(p), b = tr_read(p + second);
  return u32x4{a.x, a.y, b.x, b.y};
}
DI void swap32(unsigned& a, unsigned& b) { auto r = __builtin_amdgcn_permlane32_swap(a, b, false, false); a = r[0]; b = r[1]; }
DI void swap16(unsigned& a, unsigned& b) { auto r = __builtin_amdgcn_permlane16_swap(a, b, false, false); a = r[0]; b = r[1]; }
DI void swap32f(float& a, float& b) { unsigned x = __float_as_uint(a), y = __float_as_uint(b); swap32(x, y); a = __uint_as_float(x); b = __uint_as_float(y); }
DI void swap16f(float& a, float& b) { unsigned x = __float_as_uint(a), y = __float_as_uint(b); swap16(x, y); a = __uint_as_float(x); b = __uint_as_float(y); }
DI float max3(float a, float b, float c) { return __builtin_fmaxf(__builtin_fmaxf(a, b), c); }
DI float max2(float a, float b) { float ninf = -__builtin_inff(); asm("" : "+s"(ninf)); return max3(a, b, ninf); }
DI float sel4(f32x4 v, int i) { const float a = (i & 1) ? v.y : v.x, b = (i & 1) ? v.w : v.z; return (i & 2) ? b : a; }
DI float sum16q(float s) {
  s += __builtin_bit_cast(float, __builtin_amdgcn_update_dpp(0, __builtin_bit_cast(int, s), 0x124, 0xf, 0xf, true));
  s += __builtin_bit_cast(float, __builtin_amdgcn_update_dpp(0, __builtin_bit_cast(int, s), 0x128, 0xf, 0xf, true));
  float a = s, b = s;
  swap16f(a, b);
  s = a + b; a = s; b = s;
  swap32f(a, b);
  return a + b;
}
DI f32x4 sum16q(u32x4 v) { return f32x4{sum16q(__uint_as_float(v.x)), sum16q(__uint_as_float(v.y)), sum16q(__uint_as_float(v.z)), sum16q(__uint_as_float(v.w))}; }
DI void xsum2(float& s1, float& s2) {
  swap32f(s1, s2);
  float t = s1 + s2, u = t;
  swap16f(t, u);
  float a = t + u, b = a;
  swap32f(a, b);
  s1 = a; s2 = b;
}
#define BAR() asm volatile("s_waitcnt lgkmcnt(0)\n\ts_barrier" ::: "memory")
#define DRAIN() asm volatile("s_waitcnt vmcnt(0)" ::: "memory")
#define SB() __builtin_amdgcn_sched_barrier(0)
#define OPAQUE_V(x) asm volatile("" : "+v"(x))
#define OPAQUE_S(x) asm volatile("" : "+s"(x))

DI void dma16(const void* sbase, unsigned voff, unsigned lds_addr) {
  unsigned keep;
  asm volatile("s_mov_b32 %0, m0\n\ts_mov_b32 m0, %3\n\ts_nop 2\n\tglobal_load_lds_dwordx4 %1, %2\n\ts_mov_b32 m0, %0"
               : "=&s"(keep) : "v"(voff), "s"(sbase), "s"(lds_addr) : "memory");
}

struct Ctx {
  __amdgpu_buffer_rsrc_t rs;
  unsigned tag0, tag1;
  int lane, wave, gi, gj;
};

DI u32x4 ld128(const Ctx& x, int voff, int soff) { return __builtin_amdgcn_raw_buffer_load_b128(x.rs, voff, soff, AUX_SC1); }
DI void st128(const Ctx& x, u32x4 v, int voff, int soff) { __builtin_amdgcn_raw_buffer_store_b128(v, x.rs, voff + soff, 0, AUX_SC1); }
DI void st64(const Ctx& x, u32x2 v, int voff, int soff) { __builtin_amdgcn_raw_buffer_store_b64(v, x.rs, voff, soff, AUX_SC1); }

DI void signal(const Ctx& x, int lane, int word) {
  int so = WS_FLAGS + word * FSTR;
  u32x2 tg = {x.tag0, x.tag1};
  int vo = lane == 0 ? 0 : 0x7ffffff0;
  OPAQUE_S(so); OPAQUE_V(tg); OPAQUE_V(vo);
  DRAIN();
  st64(x, tg, vo, so);
}
constexpr int PRE_X = 12, PRE_XB = 5, PRE_OA = 12, PRE_OB = 12, PRE_KV = 8;
template <int PRE>
DI void poll(const Ctx& x, int word) {
  if (PRE) __builtin_amdgcn_s_sleep(PRE);
  for (unsigned spins = 0; spins < SPIN_CAP; ++spins) {
    asm volatile("" ::: "memory");
    const u32x2 v = __builtin_amdgcn_raw_buffer_load_b64(x.rs, word * FSTR, WS_FLAGS, AUX_SC1);
    if (__all(v.x == x.tag0 && v.y == x.tag1)) break;
  }
  __builtin_amdgcn_fence(__ATOMIC_ACQUIRE, "wavefront");
  asm volatile("" ::: "memory");
}

DI u32x4 ld_stats(const Ctx& x, int voff, int soff) {
  const u32x2 a = __builtin_amdgcn_raw_buffer_load_b64(x.rs, voff, soff, AUX_SC1), b = __builtin_amdgcn_raw_buffer_load_b64(x.rs, voff + 128, soff, AUX_SC1);
  return u32x4{a.x, a.y, b.x, b.y};
}
DI void finish_stats(u32x4 p, float& mean, float& rstd) {
  float s1 = __uint_as_float(p.x) + __uint_as_float(p.z), s2 = __uint_as_float(p.y) + __uint_as_float(p.w);
  xsum2(s1, s2);
  mean = s1 * (1.f / 512.f);
  float var = fmaxf(s2 * (1.f / 512.f) - mean * mean, 0.f);
  rstd = __builtin_amdgcn_rsqf(var + 1e-5f);
}
DI u32x2 ln_pack(f32x4 a, f32x4 cs, float mean, float rstd) {
  float v0 = fmaf(-mean, cs.x, a.x) * rstd, v1 = fmaf(-mean, cs.y, a.y) * rstd;
  float v2 = fmaf(-mean, cs.z, a.z) * rstd, v3 = fmaf(-mean, cs.w, a.w) * rstd;
  return u32x2{pk2(v0, v1), pk2(v2, v3)};
}

template <class F>
DI void feature_map(u32x4 in, const u32x4 (&wp)[4], unsigned (&o)[8], F&& filler) {
  f32x4 p[4];
  const f32x4 z = {0.f, 0.f, 0.f, 0.f};
#pragma unroll
  for (int t = 0; t < 4; ++t) p[t] = mfma32(wp[t], in, z);
  filler();
  SB();
  const float m = max3(max3(max3(p[0].x, p[0].y, p[0].z), max3(p[0].w, p[1].x, p[1].y), max3(p[1].z, p[1].w, p[2].x)),
                       max3(p[2].y, p[2].z, p[2].w), max3(max3(p[3].x, p[3].y, p[3].z), p[3].w, p[0].x));
  const float LOG2E = 1.4426950408889634f;
#pragma unroll
  for (int t = 0; t < 4; ++t) p[t] = (p[t] - m) * LOG2E;
#pragma unroll
  for (int t = 0; t < 4; ++t)
#pragma unroll
    for (int r = 0; r < 4; ++r) p[t][r] = __builtin_amdgcn_exp2f(p[t][r]);
  const f32x4 s4 = (p[0] + p[1]) + (p[2] + p[3]);
  const float rs = __builtin_amdgcn_rcpf((s4.x + s4.y) + (s4.z + s4.w));
#pragma unroll
  for (int t = 0; t < 4; ++t) { const f32x4 v = p[t] * rs; o[2 * t] = pk2(v.x, v.y); o[2 * t + 1] = pk2(v.z, v.w); }
}

constexpr int XWIN = 5;
constexpr int OWIN = 8;
constexpr int START_SLEEP = 4;
constexpr int DMA_SLEEP = 1;

struct Pre {
  f32x4 cs[6];
  f32x4 bo, bp;
  int a0, a1, a2;
  int ov;
};
template <int L>
DI void make_pre(Pre& pre, int lane, int w, int gi, int gj) {
  constexpr bool IS_B = L & 1;
  constexpr int LDS_S = (IS_B ? LDS_X : LDS_Y) + 4096 + 64;
  const int c = lane & 15, g = lane >> 4, q = c >> 2, p = c & 3;
  pre.ov = WS_OF + L * FRAG_BUF + (8 * gi + w) * 16384 + gj * 1024 + lane * 16;
  if (IS_B) {
    const float NEG = -1e30f;
    const bool noprev = (w < 2) && gi == 0;
#pragma unroll
    for (int r = 0; r < 4; ++r) {
      const int key = 4 * g + r;
      pre.bo[r] = (c - key >= 0 && c - key <= 7) ? 0.f : NEG;
      pre.bp[r] = (key >= c + 9 && !noprev) ? 0.f : NEG;
    }
    OPAQUE_V(pre.bo); OPAQUE_V(pre.bp);
    pre.a0 = LDS_S + 2048 + w * 1024 + lane * 8;
    pre.a1 = LDS_S + 10240 + (2 + w) * 1024 + c * 32 + g * 8;
    pre.a2 = LDS_S + 10240 + w * 1024 + (4 * g + q) * 32 + p * 8;
  } else {
    pre.a0 = LDS_S + g * 4096 + (w * 16 + c) * 32;
    pre.a1 = LDS_S + 16384 + (w * 16 + c) * 32 + g * 8;
    pre.a2 = LDS_S + (4 * g + q) * 32 + p * 8;
  }
  OPAQUE_V(pre.a0); OPAQUE_V(pre.a1); OPAQUE_V(pre.a2); OPAQUE_V(pre.ov);
}
#define WFRAG(s, ct) (*(LAS u32x4*)((ct) < 2 ? lds_at(wq) + (s) * 2048 + (ct) * 1024 : lds_at(wa) + (s) * 4096 + ((ct) - 2) * 1024))
template <int L>
DI void gemm_phase(const Ctx& x, f32x2& cskeep, u32x4 (&poB)[8], u32x4 (&xf)[16], f32x4 (&acc)[6], float& mean, float& rstd, u32x4& hsp, Pre& pre) {
  constexpr bool IS_B = L & 1;
  constexpr int KVW = IS_B ? LDS_Y : LDS_X, SCR = IS_B ? LDS_X : LDS_Y;
  constexpr int LDS_HF = SCR + 4096, LDS_S = SCR + 4096 + 64;
  int lane = x.lane, w = x.wave, gi = x.gi, gj = x.gj;
  OPAQUE_V(lane); OPAQUE_S(w); OPAQUE_S(gi); OPAQUE_S(gj);
  const int c = lane & 15, g = lane >> 4;
  const int LDS_CS = L < 2 ? SCR + 61440 : SCR + w * 512;
  const int tau = 8 * gi + w;
  const bool has_halo = IS_B && gi > 0;
  constexpr int KIM = LDS_S + 2048, VIMB = KIM + 8192, HP = VIMB + 10240;
  const int wa = KVW + lane * 16, wq = LDS_Z + lane * 16;
  constexpr int C0 = IS_B ? 0 : 2;
  if (L != 1) {
    *(LAS float*)lds_at(LDS_CS + lane * 4) = cskeep.x;
    *(LAS float*)lds_at(LDS_CS + 256 + lane * 4) = cskeep.y;
  } else {
    for (unsigned spins = 0; spins < SPIN_CAP; ++spins) {
      const u32x2 v = *(volatile LAS u32x2*)lds_at(SCR + 61952 + (lane % 3) * 8);
      if (__all(v.x == x.tag0 && v.y == x.tag1)) break;
    }
    asm volatile("" ::: "memory");
    cskeep = f32x2{*(volatile LAS float*)lds_at(LDS_CS + lane * 4), *(volatile LAS float*)lds_at(LDS_CS + 256 + (lane & 31) * 4)};
  }
  int fo = ((EDGE_X + L) * 128 + tau) * 16 + (lane & 7);
  if (has_halo && lane >= 8 && lane < 24) fo = ((EDGE_X + L) * 128 + 8 * gi - 2 + ((lane >> 3) & 1)) * 16 + (w < 2 ? (lane & 7) : w);
  constexpr int XW = XWIN, XB = L;
  int xbase = WS_XF + XB * FRAG_BUF + tau * 16384, hbase = WS_XF + L * FRAG_BUF + w * 2048, sbase = WS_XS + XB * XS_BUF + tau * 1024;
  const int hc = 8 + (c & 7);
  int xv = lane * 16, sv = g * 256 + c * 8;
  int hxv = has_halo ? (8 * gi - 2 + (c >> 3)) * 16384 + (g * 16 + hc) * 16 : 0x7ffffff0;
  int hsv = has_halo ? (8 * gi - 2 + w) * 1024 + g * 256 + hc * 8 : 0x7fffff00;
  u32x4 wf[2][6];
#pragma unroll
  for (int ct = C0; ct < 6; ++ct) wf[0][ct] = WFRAG(0, ct);
  OPAQUE_S(xbase); OPAQUE_S(hbase); OPAQUE_S(sbase); OPAQUE_V(xv); OPAQUE_V(sv);
  if (IS_B) { OPAQUE_V(hxv); OPAQUE_V(hsv); }
  make_pre<L>(pre, lane, w, gi, gj);
  poll<(IS_B ? PRE_XB : PRE_X)>(x, fo);
#define LDX(s) ld128(x, xv + ((s) & 3) * 1024, xbase + ((s) >> 2) * 4096)
  u32x4 hx[2], sp;
#pragma unroll
  for (int s = 0; s < XW; ++s) {
    xf[s] = LDX(s);
    if (IS_B && s == 2) {
#pragma unroll
      for (int i = 0; i < 2; ++i) hx[i] = ld128(x, hxv + i * 1024, hbase);
    }
    SB();
  }
  u32x4 hf[4];
  f32x4 hacc[4];
#pragma unroll
  for (int ct = 0; ct < 6; ++ct) acc[ct] = f32x4{0.f, 0.f, 0.f, 0.f};
  SB();
#pragma unroll
  for (int s = 0; s < 16; ++s) {
    if (s + XW < 16) xf[s + XW] = LDX(s + XW);
    if (s == 4) sp = ld_stats(x, sv, sbase);
    if (IS_B && s == 10) hsp = ld_stats(x, w < 2 ? hsv : 0x7fffff00, WS_XS + L * XS_BUF);
    if (s + 1 < 16) {
#pragma unroll
      for (int ct = C0; ct < 6; ++ct) wf[(s + 1) & 1][ct] = WFRAG(s + 1, ct);
    }
#pragma unroll
    for (int ct = C0; ct < 6; ++ct) acc[ct] = mfma32(wf[s & 1][ct], xf[s], acc[ct]);
    if (IS_B) {
      if (s == 3 || s == 4) {
#pragma unroll
        for (int i = 0; i < 4; ++i) hacc[i] = mfma32(hf[i], hx[s - 3], s == 3 ? f32x4{0.f, 0.f, 0.f, 0.f} : hacc[i]);
      }
      if (s == 2 || s == 3) {
#pragma unroll
        for (int i = 0; i < 4; ++i) hf[i] = *(LAS u32x4*)(lds_at(wa + w * 8192) + (s - 2) * 4096 + i * 1024);
      }
      if (s == 5) {
#pragma unroll
        for (int i = 0; i < 4; ++i) *(LAS f32x4*)(lds_at(HP + w * 4096 + lane * 16) + i * 1024) = hacc[i];
      }
      if (s == 7) *(volatile LAS u32x2*)lds_at(LDS_HF + w * 8) = u32x2{x.tag0, x.tag1 + L};
    }
    if (s == 15) {
#pragma unroll
      for (int ct = C0; ct < 6; ++ct) pre.cs[ct] = *(LAS f32x4*)(lds_at(LDS_CS + g * 16) + ct * 64);
    }
    if (s == 10) { OPAQUE_V(sp); finish_stats(sp, mean, rstd); }
    {
#pragma unroll
      for (int i = C0; i < 6; ++i) {
        __builtin_amdgcn_sched_group_barrier(0x008, 1, 0);
        __builtin_amdgcn_sched_group_barrier(0x100, 1, 0);
        if (s == 10) __builtin_amdgcn_sched_group_barrier(0x002, 5, 0);
        if (IS_B && s == 5) __builtin_amdgcn_sched_group_barrier(0x200, 1, 0);
      }
      if (IS_B && s >= 2 && s <= 4) {
#pragma unroll
        for (int i = 0; i < 4; ++i) {
          __builtin_amdgcn_sched_group_barrier(0x008, 1, 0);
          __builtin_amdgcn_sched_group_barrier(0x100, 1, 0);
        }
      }
    }
    SB();
  }
#undef LDX
  if (L == 1) {
    if (w < 4) {
#pragma unroll
      for (int n = 0; n < 8; ++n) poB[n] = ld128(x, lane * 16 + (n & 3) * 1024, WS_WOT + (16 + gj) * 65536 + w * 8192 + (n >> 2) * 4096);
    }
  }
}

template <int L, class F>
DI void mix_phase(const Ctx& x, const u32x4 (&wp)[4], u32x4 (&po)[8], const u32x4 (&xf)[16], f32x4 (&acc)[6], float mean, float rstd, u32x4 hsp,
                  const Pre& pre, F&& late_stores) {
  constexpr bool IS_B = L & 1;
  constexpr int KVW = IS_B ? LDS_Y : LDS_X, SCR = IS_B ? LDS_X : LDS_Y;
  constexpr int LDS_HF = SCR + 4096, LDS_S = SCR + 4096 + 64;
  int lane = x.lane, w = x.wave, gi = x.gi, gj = x.gj;
  OPAQUE_V(lane); OPAQUE_S(w); OPAQUE_S(gi); OPAQUE_S(gj);
  const int c = lane & 15, g = lane >> 4;
  const int LDS_CS = L < 2 ? SCR + 61440 : SCR + w * 512;
  const int tau = 8 * gi + w;
  const bool has_halo = IS_B && gi > 0;
  constexpr int KIM = LDS_S + 2048, VIMB = KIM + 8192, HP = VIMB + 10240;
  const int wa = KVW + lane * 16, wq = LDS_Z + lane * 16;
  constexpr int C0 = IS_B ? 0 : 2;
  const int q = c >> 2, p = c & 3;
  const u32x4 ones = {0x3f803f80u, 0x3f803f80u, 0x3f803f80u, 0x3f803f80u};
  const f32x4 (&cs)[6] = pre.cs;
#define STAGE_WO() do { _Pragma("unroll") for (int n = 0; n < 8; ++n) *(LAS u32x4*)(lds_at(KVW + w * 8192 + lane * 16) + n * 1024) = po[n]; } while (0)
  if (!IS_B) {
    constexpr int PHI = LDS_S, VIM = PHI + 16384, KVOP = VIM + 8192, ZOP = KVOP + 2048;
    {
      u32x2 kn[2];
#pragma unroll
      for (int h = 0; h < 2; ++h) kn[h] = ln_pack(acc[2 + h], cs[2 + h], mean, rstd);
      unsigned ph[8];
      feature_map(u32x4{kn[0].x, kn[0].y, kn[1].x, kn[1].y}, wp, ph, [&]() {
#pragma unroll
        for (int h = 0; h < 2; ++h) *(LAS u32x2*)(lds_at(pre.a1) + h * 4096) = ln_pack(acc[4 + h], cs[4 + h], mean, rstd);
      });
      *(LAS u32x4*)lds_at(pre.a0) = u32x4{ph[0], ph[1], ph[2], ph[3]};
      *(LAS u32x4*)(lds_at(pre.a0) + 16) = u32x4{ph[4], ph[5], ph[6], ph[7]};
    }
    constexpr int e = L >> 1;
    int kvo = WS_KV + e * KV_BUF + ((gj * 16 + gi) * 4 + w) * 1024 + lane * 16;
    int kzo = c == 0 ? WS_KZ + e * KZ_BUF + (gj * 4 + w) * 1024 + gi * 64 + g * 16 : 0x7ffffff0;
    int pa = pre.a2 + w * 4096, pb = pre.a2 + 16384 + (w >> 1) * 4096;
    u32x4 one4 = ones;
    OPAQUE_V(kvo); OPAQUE_V(kzo); OPAQUE_V(pa); OPAQUE_V(pb); OPAQUE_V(one4);
    if (w < 4) __builtin_amdgcn_s_setprio(2);
    BAR();
    if (w < 4) {
      f32x4 kv0 = {0.f, 0.f, 0.f, 0.f}, z0 = kv0;
      u32x4 a[4], b[4];
#pragma unroll
      for (int u = 0; u < 4; ++u) { a[u] = tr_frag(lds_at(pa) + u * 1024, 512); b[u] = tr_frag(lds_at(pb) + u * 1024, 512); }
      SB();
#pragma unroll
      for (int u = 0; u < 4; ++u) { kv0 = mfma32(a[u], b[u], kv0); z0 = mfma32(a[u], one4, z0); }
      st128(x, __builtin_bit_cast(u32x4, kv0), kvo, 0);
      st128(x, __builtin_bit_cast(u32x4, z0), kzo, 0);
      __builtin_amdgcn_s_setprio(0);
      if (L > 0) STAGE_WO();
      signal(x, lane, (e * 64 + gj * 4 + w) * 16 + gi);
      if (L == 0) late_stores();
    } else {
      __builtin_amdgcn_s_setprio(1);
    }
    if (L == 0) {
#pragma unroll
      for (int n = 0; n < 8; ++n) po[n] = ld128(x, lane * 16 + (n & 3) * 1024, WS_WOT + gj * 65536 + w * 8192 + (n >> 2) * 4096);
      SB();
    }
    u32x4 pq[2];
    {
      constexpr int QR = 6;
      u32x4 f[QR];
      f32x4 csq[2];
#pragma unroll
      for (int i = 0; i < QR; ++i) f[i] = WFRAG(i >> 1, i & 1);
#pragma unroll
      for (int i = 0; i < 32; ++i) {
        acc[i & 1] = mfma32(f[i % QR], xf[i >> 1], acc[i & 1]);
        if (i + QR < 32) f[i % QR] = WFRAG((i + QR) >> 1, (i + QR) & 1);
        else if (i + QR < 34) csq[i + QR - 32] = *(LAS f32x4*)(lds_at(LDS_CS + g * 16) + (i + QR - 32) * 64);
        SB();
      }
      u32x2 qn[2];
#pragma unroll
      for (int h = 0; h < 2; ++h) qn[h] = ln_pack(acc[h], csq[h], mean, rstd);
      unsigned ph[8];
      feature_map(u32x4{qn[0].x, qn[0].y, qn[1].x, qn[1].y}, wp, ph, []() {});
#pragma unroll
      for (int r = 0; r < 2; ++r) { swap16(ph[r], ph[2 + r]); swap16(ph[4 + r], ph[6 + r]); }
#pragma unroll
      for (int r = 0; r < 2; ++r) { swap32(ph[r], ph[4 + r]); swap32(ph[2 + r], ph[6 + r]); }
      pq[0] = u32x4{ph[0], ph[1], ph[2], ph[3]};
      pq[1] = u32x4{ph[4], ph[5], ph[6], ph[7]};
      OPAQUE_V(pq[0]); OPAQUE_V(pq[1]);
    }
    if (w >= 4) {
      const int wi = w - 4;
      int lv = lane * 16;
      int sbase = WS_KV + e * KV_BUF + (gj * 64 + wi) * 1024;
      int wr = KVOP + (wi >> 1) * 1024 + lane * 16 + (wi & 1) * 8;
      OPAQUE_S(sbase); OPAQUE_V(lv); OPAQUE_V(wr);
      poll<PRE_KV>(x, (e * 64 + gj * 4 + wi) * 16 + c);
      const u32x4 zu = ld128(x, lv, WS_KZ + e * KZ_BUF + (gj * 4 + wi) * 1024);
      u32x4 a[16];
#pragma unroll
      for (int i = 0; i < 16; ++i) { a[i] = ld128(x, lv, sbase + i * 4096); SB(); }
      STAGE_WO();
      const f32x4 zs = sum16q(zu);
      if (lane < 4) *(LAS u32x2*)lds_at(ZOP + (wi >> 1) * 64 + lane * 16 + (wi & 1) * 8) = u32x2{pk2(zs.x, zs.y), pk2(zs.z, zs.w)};
      f32x4 t[4];
#pragma unroll
      for (int i = 0; i < 4; ++i)
        t[i] = (__builtin_bit_cast(f32x4, a[4 * i]) + __builtin_bit_cast(f32x4, a[4 * i + 1])) + (__builtin_bit_cast(f32x4, a[4 * i + 2]) + __builtin_bit_cast(f32x4, a[4 * i + 3]));
      const f32x4 s0 = (t[0] + t[1]) + (t[2] + t[3]);
      *(LAS u32x2*)lds_at(wr) = u32x2{pk2(s0.x, s0.y), pk2(s0.z, s0.w)};
      __builtin_amdgcn_s_setprio(0);
    } else if (L == 0) {
      if (w < 3) signal(x, lane, (EDGE_SETUP * 128 + 64 + gj * 4 + w) * 16 + gi);
      STAGE_WO();
    }
    int za = ZOP + g * 16, ka = KVOP + lane * 16;
    OPAQUE_V(za); OPAQUE_V(ka);
    BAR();
    {
      const f32x4 z = {0.f, 0.f, 0.f, 0.f};
      u32x4 kvop[2], zop[2];
#pragma unroll
      for (int h = 0; h < 2; ++h) zop[h] = *(LAS u32x4*)(lds_at(za) + h * 64);
#pragma unroll
      for (int h = 0; h < 2; ++h) kvop[h] = *(LAS u32x4*)(lds_at(ka) + h * 1024);
      SB();
      f32x4 num[2], den[2];
#pragma unroll
      for (int h = 0; h < 2; ++h) den[h] = mfma32(zop[h], pq[h], z);
#pragma unroll
      for (int h = 0; h < 2; ++h) num[h] = mfma32(kvop[h], pq[h], z);
      SB();
      unsigned o[4];
#pragma unroll
      for (int h = 0; h < 2; ++h) {
        const float r = __builtin_amdgcn_rcpf(den[h].x + 1e-6f);
        o[2 * h] = pk2(num[h].x * r, num[h].y * r); o[2 * h + 1] = pk2(num[h].z * r, num[h].w * r);
      }
      st128(x, u32x4{o[0], o[1], o[2], o[3]}, pre.ov, 0);
    }
  } else {
    constexpr int VIM = VIMB;
    u32x2 qn[2], kn[2], kp[2];
#pragma unroll
    for (int h = 0; h < 2; ++h) {
      kn[h] = ln_pack(acc[2 + h], cs[2 + h], mean, rstd);
      *(LAS u32x2*)(lds_at(pre.a0) + h * 512) = kn[h];
      *(LAS u32x2*)(lds_at(pre.a1) + h * 512) = ln_pack(acc[4 + h], cs[4 + h], mean, rstd);
    }
    if (w < 2) {
      for (unsigned spins = 0; spins < SPIN_CAP; ++spins) {
        const u32x2 v = *(volatile LAS u32x2*)lds_at(LDS_HF + (lane & 7) * 8);
        if (__all(v.x == x.tag0 && v.y == x.tag1 + L)) break;
      }
      asm volatile("" ::: "memory");
      float hm, hr;
      finish_stats(hsp, hm, hr);
      const int src = HP + (c < 8 ? 2048 : 0) + (g * 16 + w * 8 + (c & 7)) * 16;
#pragma unroll
      for (int h = 0; h < 2; ++h) {
        f32x4 a[8];
#pragma unroll
        for (int i = 0; i < 8; ++i) a[i] = *(LAS f32x4*)(lds_at(src) + i * 4096 + h * 1024);
        const f32x4 sum = ((a[0] + a[1]) + (a[2] + a[3])) + ((a[4] + a[5]) + (a[6] + a[7]));
        const f32x4 csw = c < 8 ? cs[4 + h] : cs[2 + h];
        u32x2 v = ln_pack(sum, csw, hm, hr);
        if (!has_halo) v = u32x2{0u, 0u};
        kp[h] = c < 8 ? u32x2{0u, 0u} : v;
        *(LAS u32x2*)lds_at(VIM + (w * 2 + h) * 512 + (c ^ 8) * 32 + g * 8) = c < 8 ? v : u32x2{0u, 0u};
      }
    }
#pragma unroll
    for (int h = 0; h < 2; ++h) qn[h] = ln_pack(acc[h], cs[h], mean, rstd);
    f32x4 so[2];
#pragma unroll
    for (int h = 0; h < 2; ++h) so[h] = mfma16(kn[h], qn[h], pre.bo);
    if (w < 2) {
#pragma unroll
      for (int h = 0; h < 2; ++h) *(LAS u32x2*)(lds_at(pre.a0 - 2048) + h * 512) = kp[h];
    }
    SB();
    BAR();
    {
      const f32x4 z = {0.f, 0.f, 0.f, 0.f};
      const float SC = 0.25f * 1.4426950408889634f;
      f32x4 sp_[2];
      u32x2 vo[2], vp[2], kq[2];
      float m[2];
#pragma unroll
      for (int h = 0; h < 2; ++h) kq[h] = *(LAS u32x2*)(lds_at(pre.a0 - 2048) + h * 512);
#pragma unroll
      for (int h = 0; h < 2; ++h) {
        vp[h] = tr_read(lds_at(pre.a2) + h * 512);
        vo[h] = tr_read(lds_at(pre.a2) + 2048 + h * 512);
      }
      SB();
#pragma unroll
      for (int h = 0; h < 2; ++h) sp_[h] = mfma16(kq[h], qn[h], pre.bp);
#pragma unroll
      for (int h = 0; h < 2; ++h)
        m[h] = max3(max3(so[h].x, so[h].y, so[h].z), max3(so[h].w, sp_[h].x, sp_[h].y), max3(sp_[h].z, sp_[h].w, so[h].x));
      swap32f(m[0], m[1]);
      float t = max2(m[0], m[1]), u = t;
      swap16f(t, u);
      float a = max2(t, u), b = a;
      swap32f(a, b);
      m[0] = a; m[1] = b;
      u32x4 P[2];
#pragma unroll
      for (int h = 0; h < 2; ++h) {
        const f32x4 d0 = (so[h] - m[h]) * SC, d1 = (sp_[h] - m[h]) * SC;
        float e[8];
#pragma unroll
        for (int r = 0; r < 4; ++r) { e[r] = __builtin_amdgcn_exp2f(d0[r]); e[4 + r] = __builtin_amdgcn_exp2f(d1[r]); }
        P[h] = u32x4{pk2(e[0], e[1]), pk2(e[2], e[3]), pk2(e[4], e[5]), pk2(e[6], e[7])};
      }
      SB();
      f32x4 num[2], den[2];
#pragma unroll
      for (int h = 0; h < 2; ++h) den[h] = mfma32(ones, P[h], z);
#pragma unroll
      for (int h = 0; h < 2; ++h) num[h] = mfma32(u32x4{vo[h].x, vo[h].y, vp[h].x, vp[h].y}, P[h], z);
      SB();
      unsigned o[4];
#pragma unroll
      for (int h = 0; h < 2; ++h) {
        const float r = __builtin_amdgcn_rcpf(den[h].x);
        o[2 * h] = pk2(num[h].x * r, num[h].y * r); o[2 * h + 1] = pk2(num[h].z * r, num[h].w * r);
      }
      st128(x, u32x4{o[0], o[1], o[2], o[3]}, pre.ov, 0);
    }
    SB();
    STAGE_WO();
  }
  signal(x, lane, ((EDGE_O + L) * 128 + tau) * 16 + gj);
  BAR();
#undef WFRAG
#undef STAGE_WO
}

template <int L>
DI void oproj_phase(const Ctx& x, f32x4 (&res)[4], u32x4 (&poB)[8], u16* out) {
  constexpr bool IS_B = L & 1;
  constexpr int KVW = IS_B ? LDS_Y : LDS_X, SCR = IS_B ? LDS_X : LDS_Y;
  int lane = x.lane, w = x.wave, gi = x.gi, gj = x.gj;
  OPAQUE_V(lane); OPAQUE_S(w); OPAQUE_S(gi); OPAQUE_S(gj);
  const int c = lane & 15, g = lane >> 4;
  const int tau = 8 * gi + 4 * (gj >> 3) + w;
  f32x4 cb[2] = {f32x4{0.f, 0.f, 0.f, 0.f}, f32x4{0.f, 0.f, 0.f, 0.f}};
  if (w < 4) {
    const int wb = KVW + lane * 16;
    constexpr int PD = 3, OW = OWIN;
    u32x4 wf[PD][4];
#pragma unroll
    for (int s = 0; s < PD; ++s)
#pragma unroll
      for (int ct = 0; ct < 4; ++ct) wf[s][ct] = *(LAS u32x4*)(lds_at(wb) + s * 4096 + ct * 1024);
    int xo = WS_XF + (L + 1) * FRAG_BUF + (tau * 16 + (gj & 7) * 2) * 1024 + lane * 16;
    int so = g == (c >> 2) ? WS_XS + (L + 1) * XS_BUF + tau * 1024 + (gj & 7) * 128 + c * 8 : 0x7ffffff0;
    u32x4 one4 = {0x3f803f80u, 0x3f803f80u, 0x3f803f80u, 0x3f803f80u};
    int obase = WS_OF + L * FRAG_BUF + tau * 16384, lv = lane * 16;
    if (L < 3) { OPAQUE_V(xo); OPAQUE_V(so); OPAQUE_V(one4); }
    OPAQUE_S(obase); OPAQUE_V(lv);
    poll<(IS_B ? PRE_OB : PRE_OA)>(x, ((EDGE_O + L) * 128 + tau) * 16 + c);
    u32x4 of[16];
#pragma unroll
    for (int s = 0; s < OW; ++s) { of[s] = ld128(x, lv + (s & 3) * 1024, obase + (s >> 2) * 4096); SB(); }
#pragma unroll
    for (int s = 0; s < 16; ++s) {
      if (s + OW < 16) of[s + OW] = ld128(x, lv + ((s + OW) & 3) * 1024, obase + ((s + OW) >> 2) * 4096);
#pragma unroll
      for (int ct = 0; ct < 4; ++ct) {
        res[ct] = mfma32(wf[s % PD][ct], of[s], res[ct]);
        if (s + PD < 16) wf[s % PD][ct] = *(LAS u32x4*)(lds_at(wb) + (s + PD) * 4096 + ct * 1024);
      }
      SB();
    }
    const int nb = gj & 7;
    u32x4 f[2];
#pragma unroll
    for (int a = 0; a < 2; ++a)
      f[a] = u32x4{pk2(res[2 * a].x, res[2 * a].y), pk2(res[2 * a].z, res[2 * a].w), pk2(res[2 * a + 1].x, res[2 * a + 1].y), pk2(res[2 * a + 1].z, res[2 * a + 1].w)};
    if (L < 3) {
      const f32x4 z = {0.f, 0.f, 0.f, 0.f};
      const f32x4 sq = mfma32(f[1], f[1], mfma32(f[0], f[0], z)), sm = mfma32(one4, f[1], mfma32(one4, f[0], z));
#pragma unroll
      for (int a = 0; a < 2; ++a) st128(x, f[a], xo + a * 1024, 0);
      st64(x, u32x2{__float_as_uint(sm.x), __float_as_uint(sel4(sq, c & 3))}, so, 0);
      signal(x, lane, ((EDGE_X + L + 1) * 128 + tau) * 16 + nb);
    } else {
      const int tok = 32 * (tau >> 1) + 2 * c + (tau & 1);
#pragma unroll
      for (int a = 0; a < 2; ++a) {
        *(GAS u32x4*)(out + tok * 512 + nb * 64 + a * 32 + 8 * g) = f[a];
      }
    }
  } else if (L < 3) {
    if (!IS_B && L == 0) {
      poll<0>(x, (EDGE_SETUP * 128 + 64 + gj * 4) * 16 + (lane < 48 ? lane : 0));
#pragma unroll
      for (int n = 0; n < 2; ++n) cb[n] = sum16q(ld128(x, lane * 16, WS_CSP + (16 + gj) * 6144 + ((w < 7 ? w - 4 : 0) * 2 + n) * 1024));
    }
    const int src = WS_WT + ((IS_B ? 0 : 16) + gj) * 98304;
    const int wi = w - 4;
    u32x4 f[24];
#define FSRC(n) ((n) < 16 ? src + wi * 16384 + ((n) >> 2) * 4096 : src + 65536 + wi * 8192 + (((n) - 16) >> 2) * 4096)
#define FDST(n) ((n) < 16 ? lds_at(SCR + wi * 16384 + lane * 16) + (n) * 1024 : lds_at(LDS_Z + wi * 8192 + lane * 16) + ((n) - 16) * 1024)
#pragma unroll
    for (int n = 0; n < 24; ++n) {
      f[n] = ld128(x, lane * 16 + (n & 3) * 1024, FSRC(n));
      __builtin_amdgcn_s_sleep(DMA_SLEEP);
      if (n >= 6) *(LAS u32x4*)FDST(n - 6) = f[n - 6];
      SB();
    }
#pragma unroll
    for (int n = 18; n < 24; ++n) *(LAS u32x4*)FDST(n) = f[n];
#undef FSRC
#undef FDST
  }
  if (!IS_B && L == 0) {
    if (w >= 4) {
#pragma unroll
      for (int n = 0; n < 8; ++n) poB[n] = ld128(x, lane * 16 + (n & 3) * 1024, WS_WOT + (16 + gj) * 65536 + w * 8192 + (n >> 2) * 4096);
    }
  }
  if (L < 3) BAR();
  if (L == 0 && w >= 4 && w < 7) {
    if (lane < 4) {
#pragma unroll
      for (int n = 0; n < 2; ++n) *(LAS f32x4*)lds_at(KVW + 61440 + ((w - 4) * 2 + n) * 64 + lane * 16) = cb[n];
    }
    *(volatile LAS u32x2*)lds_at(KVW + 61952 + (w - 4) * 8) = u32x2{x.tag0, x.tag1};
  }
}

__global__ void __launch_bounds__(512, 2)
fused_fwd(const u16* __restrict__ x0, const u16* __restrict__ wA, const u16* __restrict__ wphi, const u16* __restrict__ woA,
          const u16* __restrict__ wB, const u16* __restrict__ woB, u16* __restrict__ out, unsigned char* __restrict__ wsp) {
  extern __shared__ __attribute__((aligned(16))) unsigned char smem[];
  asm volatile("" : "+s"(x0), "+s"(wA), "+s"(wphi), "+s"(woA), "+s"(wB), "+s"(woB), "+s"(out), "+s"(wsp));
  Ctx x;
  x.lane = threadIdx.x & 63;
  x.wave = __builtin_amdgcn_readfirstlane(threadIdx.x >> 6);
  {
    constexpr int NJ = MAP_NJ, NI = 32 / NJ, XJ = 16 / NJ;
    const int xc = blockIdx.x & 7, r = blockIdx.x >> 3;
    x.gi = (xc / XJ) * NI + r / NJ;
    x.gj = (xc % XJ) * NJ + r % NJ;
  }
  x.rs = __builtin_amdgcn_make_buffer_rsrc((void*)wsp, 0, WS_END, 0x00020000);
  const int lane = x.lane, w = x.wave, c = lane & 15, g = lane >> 4, q = c >> 2, p = c & 3, gi = x.gi, gj = x.gj;
  const u32x4 ones = {0x3f803f80u, 0x3f803f80u, 0x3f803f80u, 0x3f803f80u};
  const int tau0 = 8 * gi + w;
  const int xstg = (w < 4 ? LDS_Y : LDS_X) + (w & 3) * 16384;
  f32x4 res[4];
  auto unit_dma = [&](int u, int slot) {
    const bool isw = u < 6;
    const char* src = isw ? (const char*)(u < 3 ? wA : wB) + (size_t)gi * 98304 + (u % 3) * 1024 + gj * 64
                          : (const char*)(u < 8 ? woA : woB) + (size_t)gi * 32768 + (gj & 7) * 128 + (u & 1) * 64;
    const int ld = isw ? 3072 : 1024;
    const unsigned vo = (lane >> 2) * ld + (lane & 3) * 16;
    dma16(src, vo, slot);
    dma16(src + 16 * ld, vo, slot + 1024);
  };
  auto unit_read = [&](int u, int slot, u32x4 (&f)[2]) {
    const bool isw = u < 6;
    const int ad = slot + (isw ? (8 * g + q) * 64 + p * 8 : (4 * g + q) * 64 + p * 16);
    const int sec = isw ? 256 : 1024, astr = isw ? 32 : 8;
#pragma unroll
    for (int a = 0; a < 2; ++a) {
      const u32x2 lo = tr_read(lds_at(ad + a * astr)), hi = tr_read(lds_at(ad + a * astr + sec));
      f[a] = u32x4{lo.x, lo.y, hi.x, hi.y};
    }
  };
  auto unit_store = [&](int u, const u32x4 (&f)[2]) {
    const bool isw = u < 6;
    const int s3 = u % 3;
    const int dst = isw ? WS_WT + ((u / 3) * 16 + gj) * 98304 + (s3 == 0 ? 65536 + gi * 2048 : gi * 4096 + (s3 - 1) * 2048)
                        : WS_WOT + (((u - 6) >> 1) * 16 + gj) * 65536 + gi * 4096 + (u & 1) * 2048;
#pragma unroll
    for (int a = 0; a < 2; ++a) st128(x, f[a], lane * 16 + a * 1024, dst);
    if (isw) {
      const f32x4 s0 = mfma32(f[0], ones, f32x4{0.f, 0.f, 0.f, 0.f}), s1 = mfma32(f[1], ones, f32x4{0.f, 0.f, 0.f, 0.f});
      if (c < 2) st128(x, __builtin_bit_cast(u32x4, c ? s1 : s0), g * 16 + c * 1024, WS_CSP + (((u / 3) * 16 + gj) * 6 + s3 * 2) * 1024 + gi * 64);
    }
  };
  auto load_tag = [&]() {
    unsigned long long t;
    asm volatile("s_load_dwordx2 %0, %1, 0x0\n\ts_waitcnt lgkmcnt(0)" : "=s"(t) : "s"(wsp + WS_EPOCH) : "memory");
    unsigned long long z = t + 0x9E3779B97F4A7C15ull;
    z = (z ^ (z >> 30)) * 0xBF58476D1CE4E5B9ull;
    z = (z ^ (z >> 27)) * 0x94D049BB133111EBull;
    z ^= z >> 31;
    if (z == t) z = ~z;
    x.tag0 = (unsigned)z; x.tag1 = (unsigned)(z >> 32);
  };
  const int zs = LDS_Z + (w & 3) * 8192;
  if (w < 4) {
    const int u0 = w < 3 ? w : 6;
    unit_dma(u0, zs);
    if (w == 3) unit_dma(7, zs + 2048);
    load_tag();
    DRAIN();
    u32x4 f[2];
    unit_read(u0, zs, f);
    unit_store(u0, f);
    if (w == 3) { unit_read(7, zs + 2048, f); unit_store(7, f); }
    signal(x, lane, (EDGE_SETUP * 128 + gj * 4 + w) * 16 + gi);
  } else {
    if (w == 7) {
      unsigned long long pc; unsigned len;
      asm volatile("s_getpc_b64 %0\n.Lfused_fwd_here:\n\ts_mov_b32 %1, .Lfused_fwd_end-.Lfused_fwd_here" : "=s"(pc), "=s"(len));
      const unsigned off = ((blockIdx.x >> 3) + 32 * (lane & 15)) * 128;
      dma16((const void*)(pc & ~15ull), off + 16 <= len && (int)len > 0 ? off : 0u, zs + 4096);
    }
    __builtin_amdgcn_s_sleep(START_SLEEP);
  }
  {
    const char* xrow = (const char*)x0 + (32 * (tau0 >> 1) + (tau0 & 1)) * 1024;
#pragma unroll
    for (int n = 0; n < 16; ++n) dma16(xrow + n * 2048, (lane ^ (n << 2)) << 4, xstg + n * 1024);
  }
  if (w < 4) {
    if (w < 3) unit_dma(3 + w, zs + 2048);
    if (w < 2) unit_dma(8 + w, zs + 4096);
    const int tau = 8 * gi + 4 * (gj >> 3) + w;
    const unsigned rvo = (32 * (tau >> 1) + 2 * (lane >> 3) + (tau & 1)) * 1024 + ((lane & 7) << 4);
    dma16((const char*)x0 + (gj & 7) * 128, rvo, zs + 6144);
    dma16((const char*)x0 + (gj & 7) * 128 + 16384, rvo, zs + 7168);
    dma16((const char*)wphi + gj * 1024, lane * 16, zs);
  }
  u32x4 wp[4];
  auto read_wp = [&]() {
    const unsigned sg = (q & 1) ? 0x80008000u : 0u;
#pragma unroll
    for (int t = 0; t < 4; ++t) {
      const u32x2 v = tr_read(lds_at(zs + (p >> 1) * 512 + (4 * g + q) * 32) + t * 8);
      wp[t] = (q >> 1) ? u32x4{0u, 0u, v.x ^ sg, v.y ^ sg} : u32x4{v.x ^ sg, v.y ^ sg, 0u, 0u};
    }
    OPAQUE_V(wp[0]); OPAQUE_V(wp[1]); OPAQUE_V(wp[2]); OPAQUE_V(wp[3]);
  };
  if (w >= 4) load_tag();
  DRAIN();
  u32x4 xf[16], fd0[2], fd1[2];
#pragma unroll
  for (int s = 0; s < 16; ++s) xf[s] = *(LAS u32x4*)lds_at(xstg + c * 1024 + ((s ^ c) << 6) + g * 16);
  unit_read(3, zs + 2048, fd0);
  unit_read(8, zs + 4096, fd1);
#pragma unroll
  for (int a = 0; a < 2; ++a) {
    const u32x4 v = *(LAS u32x4*)lds_at(zs + 6144 + c * 128 + ((a * 4 + g) << 4));
    res[2 * a] = f32x4{bflo(v.x), bfhi(v.x), bflo(v.y), bfhi(v.y)};
    res[2 * a + 1] = f32x4{bflo(v.z), bfhi(v.z), bflo(v.w), bfhi(v.w)};
  }
  asm volatile("s_waitcnt lgkmcnt(0)" : "+v"(xf[0]), "+v"(xf[1]), "+v"(xf[2]), "+v"(xf[3]), "+v"(xf[4]), "+v"(xf[5]), "+v"(xf[6]), "+v"(xf[7]),
               "+v"(xf[8]), "+v"(xf[9]), "+v"(xf[10]), "+v"(xf[11]), "+v"(xf[12]), "+v"(xf[13]), "+v"(xf[14]), "+v"(xf[15]),
               "+v"(fd0[0]), "+v"(fd0[1]), "+v"(fd1[0]), "+v"(fd1[1]), "+v"(res[0]), "+v"(res[1]), "+v"(res[2]), "+v"(res[3]) :: "memory");
  float mean, rstd;
  auto row_stats = [&]() {
    f32x4 accs = {0.f, 0.f, 0.f, 0.f}, accq = accs;
#pragma unroll
    for (int n = 0; n < 16; ++n) { accs = mfma32(ones, xf[n], accs); accq = mfma32(xf[n], xf[n], accq); }
    const float dq = sel4(accq, p);
    finish_stats(u32x4{__float_as_uint(accs.x * 0.25f), g == q ? __float_as_uint(dq) : 0u, 0u, 0u}, mean, rstd);
    OPAQUE_V(mean); OPAQUE_V(rstd);
  };
  row_stats();
  u32x4 fq[4];
  f32x4 cb[2];
  if (w >= 4) {
    poll<0>(x, (EDGE_SETUP * 128 + gj * 4) * 16 + lane);
    u32x4 f[16], cp[2];
#pragma unroll
    for (int n = 0; n < 16; ++n) { f[n] = ld128(x, lane * 16 + (n & 3) * 1024, WS_WT + gj * 98304 + (w - 4) * 16384 + (n >> 2) * 4096); SB(); }
#pragma unroll
    for (int n = 0; n < 2; ++n) cp[n] = ld128(x, lane * 16, WS_CSP + gj * 6144 + ((w < 7 ? w - 4 : 0) * 2 + n) * 1024);
    SB();
#pragma unroll
    for (int n = 0; n < 16; ++n) *(LAS u32x4*)(lds_at(xstg + lane * 16) + n * 1024) = f[n];
#pragma unroll
    for (int n = 0; n < 2; ++n) cb[n] = sum16q(cp[n]);
  } else {
    cb[0] = f32x4{0.f, 0.f, 0.f, 0.f}; cb[1] = cb[0];
    read_wp();
  }
  BAR();
  if (w >= 4) {
    read_wp();
    if (w < 7 && lane < 4) {
#pragma unroll
      for (int n = 0; n < 2; ++n) *(LAS f32x4*)lds_at(LDS_Y + 61440 + ((w - 4) * 2 + n) * 64 + lane * 16) = cb[n];
    }
    if (w < 7) *(volatile LAS u32x2*)lds_at(LDS_Y + 61952 + (w - 4) * 8) = u32x2{x.tag0, x.tag1};
  }
  Pre pre;
  make_pre<0>(pre, lane, w, gi, gj);
  u32x2 rdy;
  const int qsl = w >= 4 ? (w - 4) * 2 : w * 2 + 1;
  u32x4 poA[8], poB[8];
  f32x4 acc[6];
#pragma unroll
  for (int ct = 0; ct < 6; ++ct) acc[ct] = f32x4{0.f, 0.f, 0.f, 0.f};
  {
    u32x4 wf[2][4];
#pragma unroll
    for (int ct = 0; ct < 4; ++ct) wf[0][ct] = *(LAS u32x4*)(lds_at(LDS_X + lane * 16) + ct * 1024);
    SB();
#pragma unroll
    for (int s = 0; s < 16; ++s) {
      if (s + 1 < 16) {
#pragma unroll
        for (int ct = 0; ct < 4; ++ct) wf[(s + 1) & 1][ct] = *(LAS u32x4*)(lds_at(LDS_X + lane * 16) + (s + 1) * 4096 + ct * 1024);
      }
#pragma unroll
      for (int ct = 0; ct < 4; ++ct) acc[2 + ct] = mfma32(wf[s & 1][ct], xf[s], acc[2 + ct]);
      if (s >= 1 && s < 5) fq[s - 1] = ld128(x, lane * 16 + (s - 1) * 1024, WS_WT + gj * 98304 + 65536 + qsl * 4096);
      if (s == 12) rdy = *(volatile LAS u32x2*)lds_at(LDS_Y + 61952 + (lane % 3) * 8);
      if (s + 1 < 16) {
#pragma unroll
        for (int i = 0; i < 4; ++i) {
          __builtin_amdgcn_sched_group_barrier(0x008, 1, 0);
          __builtin_amdgcn_sched_group_barrier(0x100, 1, 0);
        }
      }
      SB();
    }
  }
#pragma unroll
  for (int n = 0; n < 4; ++n) *(LAS u32x4*)(lds_at(LDS_Z + qsl * 4096 + lane * 16) + n * 1024) = fq[n];
  if (__builtin_expect(!__all(rdy.x == x.tag0 && rdy.y == x.tag1), 0)) {
    for (unsigned spins = 0; spins < SPIN_CAP; ++spins) {
      const u32x2 v = *(volatile LAS u32x2*)lds_at(LDS_Y + 61952 + (lane % 3) * 8);
      if (__all(v.x == x.tag0 && v.y == x.tag1)) break;
    }
  }
  asm volatile("" ::: "memory");
  f32x2 csA = {*(volatile LAS float*)lds_at(LDS_Y + 61440 + lane * 4), *(volatile LAS float*)lds_at(LDS_Y + 61440 + 256 + (lane & 31) * 4)};
  f32x2 csB = {0.f, 0.f};
  u32x4 hsp = {0u, 0u, 0u, 0u};
#pragma unroll
  for (int ct = 2; ct < 6; ++ct) pre.cs[ct] = *(volatile LAS f32x4*)(lds_at(LDS_Y + 61440 + g * 16) + ct * 64);
  auto none = []() {};
  mix_phase<0>(x, wp, poA, xf, acc, mean, rstd, hsp, pre, [&]() {
    if (w < 3) unit_store(3 + w, fd0);
    if (w < 2) unit_store(8 + w, fd1);
  });
  oproj_phase<0>(x, res, poB, out);
  if (blockIdx.x == 0 && threadIdx.x == 0) st64(x, u32x2{x.tag0, x.tag1}, 0, WS_EPOCH);
  gemm_phase<1>(x, csB, poB, xf, acc, mean, rstd, hsp, pre);
  mix_phase<1>(x, wp, poB, xf, acc, mean, rstd, hsp, pre, none);
  oproj_phase<1>(x, res, poB, out);
  gemm_phase<2>(x, csA, poB, xf, acc, mean, rstd, hsp, pre);
  mix_phase<2>(x, wp, poA, xf, acc, mean, rstd, hsp, pre, none);
  oproj_phase<2>(x, res, poB, out);
  gemm_phase<3>(x, csB, poB, xf, acc, mean, rstd, hsp, pre);
  mix_phase<3>(x, wp, poB, xf, acc, mean, rstd, hsp, pre, none);
  oproj_phase<3>(x, res, poB, out);
  asm volatile(".Lfused_fwd_end:" ::: "memory");
}

extern "C" void kernel_launch(void* const* d_in, const int* in_sizes, int n_in, void* d_out, int out_size, void* d_ws, size_t ws_size,
                              hipStream_t stream) {
  (void)in_sizes; (void)n_in; (void)out_size; (void)ws_size;
  (void)hipFuncSetAttribute((const void*)fused_fwd, hipFuncAttributeMaxDynamicSharedMemorySize, LDS_BYTES);
  fused_fwd<<<256, 512, LDS_BYTES, stream>>>((const u16*)d_in[0], (const u16*)d_in[1], (const u16*)d_in[2], (const u16*)d_in[3],
                                             (const u16*)d_in[5], (const u16*)d_in[6], (u16*)d_out, (unsigned char*)d_ws);
}
```

```cpp
#include <hip/hip_runtime.h>
#include <stdint.h>

typedef unsigned short u16;
typedef __bf16 bf16x8 __attribute__((ext_vector_type(8)));
typedef __bf16 bf16x2 __attribute__((ext_vector_type(2)));
typedef short s16x4 __attribute__((ext_vector_type(4)));
typedef float f32x4 __attribute__((ext_vector_type(4)));
typedef float f32x2 __attribute__((ext_vector_type(2)));
typedef unsigned u32x2 __attribute__((ext_vector_type(2)));
typedef unsigned u32x4 __attribute__((ext_vector_type(4)));
#define LAS __attribute__((address_space(3)))
#define GAS __attribute__((address_space(1)))
#define DI __device__ __forceinline__

constexpr int WS_EPOCH = (26 << 20) - 64;
constexpr int FSTR = 64;
constexpr int WS_FLAGS = 24 << 20;
constexpr int EDGE_X = 0, EDGE_O = 4, EDGE_SETUP = 8;
constexpr int XS_BUF = 128 * 1024;
constexpr int WS_XS = 262144 - XS_BUF;
constexpr int WS_KZ = WS_XS + 4 * XS_BUF;
constexpr int KZ_BUF = 16 * 4 * 1024;
constexpr int WS_CSP = WS_KZ + 2 * KZ_BUF;
constexpr int FRAG_BUF = 2 << 20;
constexpr int WS_KV = 1 << 20;
constexpr int KV_BUF = 16 * 16 * 4 * 1024;
constexpr int WS_XF = WS_KV + 2 * KV_BUF - FRAG_BUF;
constexpr int WS_OF = WS_XF + 4 * FRAG_BUF;
constexpr int WS_WT = WS_OF + 4 * FRAG_BUF;
constexpr int WS_WOT = WS_WT + 2 * 16 * 98304;
constexpr int WS_END = 26 << 20;
static_assert(WS_WOT + 2 * 16 * 65536 <= WS_FLAGS && WS_FLAGS + 9 * 128 * 16 * FSTR <= WS_EPOCH && WS_CSP + 2 * 16 * 6144 <= WS_KV, "workspace map");

constexpr int LDS_X = 0, LDS_Y = 65536, LDS_Z = 131072;
constexpr int LDS_BYTES = 163840;
constexpr int AUX_SC1 = 16;
constexpr int MAP_NJ = 8;
constexpr unsigned SPIN_CAP = 1u << 16;

DI unsigned pk2(float a, float b) { f32x2 v = {a, b}; return __builtin_bit_cast(unsigned, __builtin_convertvector(v, bf16x2)); }
DI float bflo(unsigned u) { return __uint_as_float(u << 16); }
DI float bfhi(unsigned u) { return __uint_as_float(u & 0xffff0000u); }
DI f32x4 mfma32(u32x4 a, u32x4 b, f32x4 c) {
  return __builtin_amdgcn_mfma_f32_16x16x32_bf16(__builtin_bit_cast(bf16x8, a), __builtin_bit_cast(bf16x8, b), c, 0, 0, 0);
}
DI f32x4 mfma16(u32x2 a, u32x2 b, f32x4 c) {
  return __builtin_amdgcn_mfma_f32_16x16x16bf16_1k(__builtin_bit_cast(s16x4, a), __builtin_bit_cast(s16x4, b), c, 0, 0, 0);
}
DI LAS unsigned char* lds_at(int off) { return (LAS unsigned char*)(uintptr_t)(unsigned)off; }
DI u32x2 tr_read(LAS unsigned char* p) { return __builtin_bit_cast(u32x2, __builtin_amdgcn_ds_read_tr16_b64_v4i16((LAS s16x4*)p)); }
DI u32x4 tr_frag(LAS unsigned char* p, int second) {
  u32x2 a = tr_read(p), b = tr_read(p + second);
  return u32x4{a.x, a.y, b.x, b.y};
}
DI void swap32(unsigned& a, unsigned& b) { auto r = __builtin_amdgcn_permlane32_swap(a, b, false, false); a = r[0]; b = r[1]; }
DI void swap16(unsigned& a, unsigned& b) { auto r = __builtin_amdgcn_permlane16_swap(a, b, false, false); a = r[0]; b = r[1]; }
DI void swap32f(float& a, float& b) { unsigned x = __float_as_uint(a), y = __float_as_uint(b); swap32(x, y); a = __uint_as_float(x); b = __uint_as_float(y); }
DI void swap16f(float& a, float& b) { unsigned x = __float_as_uint(a), y = __float_as_uint(b); swap16(x, y); a = __uint_as_float(x); b = __uint_as_float(y); }
DI float max3(float a, float b, float c) { return __builtin_fmaxf(__builtin_fmaxf(a, b), c); }
DI float max2(float a, float b) { float ninf = -__builtin_inff(); asm("" : "+s"(ninf)); return max3(a, b, ninf); }
DI float sel4(f32x4 v, int i) { const float a = (i & 1) ? v.y : v.x, b = (i & 1) ? v.w : v.z; return (i & 2) ? b : a; }
DI float sum16q(float s) {
  s += __builtin_bit_cast(float, __builtin_amdgcn_update_dpp(0, __builtin_bit_cast(int, s), 0x124, 0xf, 0xf, true));
  s += __builtin_bit_cast(float, __builtin_amdgcn_update_dpp(0, __builtin_bit_cast(int, s), 0x128, 0xf, 0xf, true));
  float a = s, b = s;
  swap16f(a, b);
  s = a + b; a = s; b = s;
  swap32f(a, b);
  return a + b;
}
DI f32x4 sum16q(u32x4 v) { return f32x4{sum16q(__uint_as_float(v.x)), sum16q(__uint_as_float(v.y)), sum16q(__uint_as_float(v.z)), sum16q(__uint_as_float(v.w))}; }
DI void xsum2(float& s1, float& s2) {
  swap32f(s1, s2);
  float t = s1 + s2, u = t;
  swap16f(t, u);
  float a = t + u, b = a;
  swap32f(a, b);
  s1 = a; s2 = b;
}
#define BAR() asm volatile("s_waitcnt lgkmcnt(0)\n\ts_barrier" ::: "memory")
#define DRAIN() asm volatile("s_waitcnt vmcnt(0)" ::: "memory")
#define SB() __builtin_amdgcn_sched_barrier(0)
#define OPAQUE_V(x) asm volatile("" : "+v"(x))
#define OPAQUE_S(x) asm volatile("" : "+s"(x))

DI void dma16(const void* sbase, unsigned voff, unsigned lds_addr) {
  unsigned keep;
  asm volatile("s_mov_b32 %0, m0\n\ts_mov_b32 m0, %3\n\ts_nop 2\n\tglobal_load_lds_dwordx4 %1, %2\n\ts_mov_b32 m0, %0"
               : "=&s"(keep) : "v"(voff), "s"(sbase), "s"(lds_addr) : "memory");
}

struct Ctx {
  __amdgpu_buffer_rsrc_t rs;
  unsigned tag0, tag1;
  int lane, wave, gi, gj;
};

DI u32x4 ld128(const Ctx& x, int voff, int soff) { return __builtin_amdgcn_raw_buffer_load_b128(x.rs, voff, soff, AUX_SC1); }
DI void st128(const Ctx& x, u32x4 v, int voff, int soff) { __builtin_amdgcn_raw_buffer_store_b128(v, x.rs, voff + soff, 0, AUX_SC1); }
DI void st64(const Ctx& x, u32x2 v, int voff, int soff) { __builtin_amdgcn_raw_buffer_store_b64(v, x.rs, voff, soff, AUX_SC1); }

DI void signal(const Ctx& x, int lane, int word) {
  int so = WS_FLAGS + word * FSTR;
  u32x2 tg = {x.tag0, x.tag1};
  int vo = lane < FSTR / 8 ? lane * 8 : 0x7ffffff0;
  OPAQUE_S(so); OPAQUE_V(tg); OPAQUE_V(vo);
  DRAIN();
  st64(x, tg, vo, so);
}
constexpr int PRE_X = 12, PRE_XB = 4, PRE_OA = 12, PRE_OB = 12, PRE_KV = 8;
template <int PRE>
DI void poll(const Ctx& x, int word) {
  if (PRE) __builtin_amdgcn_s_sleep(PRE);
  for (unsigned spins = 0; spins < SPIN_CAP; ++spins) {
    asm volatile("" ::: "memory");
    const u32x2 v = __builtin_amdgcn_raw_buffer_load_b64(x.rs, word * FSTR, WS_FLAGS, AUX_SC1);
    if (__all(v.x == x.tag0 && v.y == x.tag1)) break;
  }
  __builtin_amdgcn_fence(__ATOMIC_ACQUIRE, "wavefront");
  asm volatile("" ::: "memory");
}

DI u32x4 ld_stats(const Ctx& x, int voff, int soff) {
  const u32x2 a = __builtin_amdgcn_raw_buffer_load_b64(x.rs, voff, soff, AUX_SC1), b = __builtin_amdgcn_raw_buffer_load_b64(x.rs, voff + 128, soff, AUX_SC1);
  return u32x4{a.x, a.y, b.x, b.y};
}
DI void finish_stats(u32x4 p, float& mean, float& rstd) {
  float s1 = __uint_as_float(p.x) + __uint_as_float(p.z), s2 = __uint_as_float(p.y) + __uint_as_float(p.w);
  xsum2(s1, s2);
  mean = s1 * (1.f / 512.f);
  float var = fmaxf(s2 * (1.f / 512.f) - mean * mean, 0.f);
  rstd = __builtin_amdgcn_rsqf(var + 1e-5f);
}
DI u32x2 ln_pack(f32x4 a, f32x4 cs, float mean, float rstd) {
  float v0 = fmaf(-mean, cs.x, a.x) * rstd, v1 = fmaf(-mean, cs.y, a.y) * rstd;
  float v2 = fmaf(-mean, cs.z, a.z) * rstd, v3 = fmaf(-mean, cs.w, a.w) * rstd;
  return u32x2{pk2(v0, v1), pk2(v2, v3)};
}

template <class F>
DI void feature_map(u32x4 in, const u32x4 (&wp)[4], unsigned (&o)[8], F&& filler) {
  f32x4 p[4];
  const f32x4 z = {0.f, 0.f, 0.f, 0.f};
#pragma unroll
  for (int t = 0; t < 4; ++t) p[t] = mfma32(wp[t], in, z);
  filler();
  SB();
  const float m = max3(max3(max3(p[0].x, p[0].y, p[0].z), max3(p[0].w, p[1].x, p[1].y), max3(p[1].z, p[1].w, p[2].x)),
                       max3(p[2].y, p[2].z, p[2].w), max3(max3(p[3].x, p[3].y, p[3].z), p[3].w, p[0].x));
  const float LOG2E = 1.4426950408889634f;
#pragma unroll
  for (int t = 0; t < 4; ++t) p[t] = (p[t] - m) * LOG2E;
#pragma unroll
  for (int t = 0; t < 4; ++t)
#pragma unroll
    for (int r = 0; r < 4; ++r) p[t][r] = __builtin_amdgcn_exp2f(p[t][r]);
  const f32x4 s4 = (p[0] + p[1]) + (p[2] + p[3]);
  const float rs = __builtin_amdgcn_rcpf((s4.x + s4.y) + (s4.z + s4.w));
#pragma unroll
  for (int t = 0; t < 4; ++t) { const f32x4 v = p[t] * rs; o[2 * t] = pk2(v.x, v.y); o[2 * t + 1] = pk2(v.z, v.w); }
}

constexpr int XWIN = 5, OWIN = 8;
constexpr int HS0 = 0, HS1 = 4, HS2 = 7;
constexpr int DMA_SLEEP = 1;

struct Pre {
  f32x4 cs[6];
  f32x4 bo, bp;
  int a0, a1, a2;
  int ov;
};
template <int L>
DI void make_pre(Pre& pre, int lane, int w, int gi, int gj) {
  constexpr bool IS_B = L & 1;
  constexpr int LDS_S = (IS_B ? LDS_X : LDS_Y) + 4096 + 64;
  const int c = lane & 15, g = lane >> 4, q = c >> 2, p = c & 3;
  pre.ov = WS_OF + L * FRAG_BUF + (8 * gi + w) * 16384 + gj * 1024 + lane * 16;
  if (IS_B) {
    const float NEG = -1e30f;
    const bool noprev = (w < 2) && gi == 0;
#pragma unroll
    for (int r = 0; r < 4; ++r) {
      const int key = 4 * g + r;
      pre.bo[r] = (c - key >= 0 && c - key <= 7) ? 0.f : NEG;
      pre.bp[r] = (key >= c + 9 && !noprev) ? 0.f : NEG;
    }
    OPAQUE_V(pre.bo); OPAQUE_V(pre.bp);
    pre.a0 = LDS_S + 2048 + w * 1024 + lane * 8;
    pre.a1 = LDS_S + 10240 + (2 + w) * 1024 + c * 32 + g * 8;
    pre.a2 = LDS_S + 10240 + w * 1024 + (4 * g + q) * 32 + p * 8;
  } else {
    pre.a0 = LDS_S + g * 4096 + (w * 16 + c) * 32;
    pre.a1 = LDS_S + 16384 + (w * 16 + c) * 32 + g * 8;
    pre.a2 = LDS_S + (4 * g + q) * 32 + p * 8;
  }
  OPAQUE_V(pre.a0); OPAQUE_V(pre.a1); OPAQUE_V(pre.a2); OPAQUE_V(pre.ov);
}
#define WFRAG(s, ct) (*(LAS u32x4*)((ct) < 2 ? lds_at(wq) + (s) * 2048 + (ct) * 1024 : lds_at(wa) + (s) * 4096 + ((ct) - 2) * 1024))
template <int L>
DI void gemm_phase(const Ctx& x, f32x2& cskeep, u32x4 (&poB)[8], u32x4 (&xf)[16], f32x4 (&acc)[6], float& mean, float& rstd, u32x4& hsp, Pre& pre) {
  constexpr bool IS_B = L & 1;
  constexpr int KVW = IS_B ? LDS_Y : LDS_X, SCR = IS_B ? LDS_X : LDS_Y;
  constexpr int LDS_HF = SCR + 4096, LDS_S = SCR + 4096 + 64;
  int lane = x.lane, w = x.wave, gi = x.gi, gj = x.gj;
  OPAQUE_V(lane); OPAQUE_S(w); OPAQUE_S(gi); OPAQUE_S(gj);
  const int c = lane & 15, g = lane >> 4;
  const int LDS_CS = L < 2 ? SCR + 61440 : SCR + w * 512;
  const int tau = 8 * gi + w;
  const bool has_halo = IS_B && gi > 0;
  constexpr int KIM = LDS_S + 2048, VIMB = KIM + 8192, HP = VIMB + 10240;
  const int wa = KVW + lane * 16, wq = LDS_Z + lane * 16;
  constexpr int C0 = IS_B ? 0 : 2;
  if (L != 1) {
    *(LAS float*)lds_at(LDS_CS + lane * 4) = cskeep.x;
    *(LAS float*)lds_at(LDS_CS + 256 + lane * 4) = cskeep.y;
  } else {
    for (unsigned spins = 0; spins < SPIN_CAP; ++spins) {
      const u32x2 v = *(volatile LAS u32x2*)lds_at(SCR + 61952 + (lane % 3) * 8);
      if (__all(v.x == x.tag0 && v.y == x.tag1)) break;
    }
    asm volatile("" ::: "memory");
    cskeep = f32x2{*(volatile LAS float*)lds_at(LDS_CS + lane * 4), *(volatile LAS float*)lds_at(LDS_CS + 256 + (lane & 31) * 4)};
  }
  int fo = ((EDGE_X + L) * 128 + tau) * 16 + (lane & 7);
  int fh = has_halo ? ((EDGE_X + L) * 128 + 8 * gi - 2 + ((lane >> 3) & 1)) * 16 + (w < 2 ? (lane & 7) : w) : fo;
  if (IS_B) OPAQUE_V(fh);
  constexpr int XW = XWIN, XB = L;
  int xbase = WS_XF + XB * FRAG_BUF + tau * 16384, hbase = WS_XF + L * FRAG_BUF + w * 2048, sbase = WS_XS + XB * XS_BUF + tau * 1024;
  const int hc = 8 + (c & 7);
  int xv = lane * 16, sv = g * 256 + c * 8;
  int hxv = has_halo ? (8 * gi - 2 + (c >> 3)) * 16384 + (g * 16 + hc) * 16 : 0x7ffffff0;
  int hsv = has_halo ? (8 * gi - 2 + w) * 1024 + g * 256 + hc * 8 : 0x7fffff00;
  u32x4 wf[2][6];
#pragma unroll
  for (int ct = C0; ct < 6; ++ct) wf[0][ct] = WFRAG(0, ct);
  OPAQUE_S(xbase); OPAQUE_S(hbase); OPAQUE_S(sbase); OPAQUE_V(xv); OPAQUE_V(sv);
  if (IS_B) { OPAQUE_V(hxv); OPAQUE_V(hsv); }
  make_pre<L>(pre, lane, w, gi, gj);
  poll<(IS_B ? PRE_XB : PRE_X)>(x, fo);
#define LDX(s) ld128(x, xv + ((s) & 3) * 1024, xbase + ((s) >> 2) * 4096)
  u32x4 hx[2], sp;
#pragma unroll
  for (int s = 0; s < XW; ++s) {
    xf[s] = LDX(s);
    SB();
  }
  u32x2 hfl;
  u32x4 hf[4];
  f32x4 hacc[4];
#pragma unroll
  for (int ct = 0; ct < 6; ++ct) acc[ct] = f32x4{0.f, 0.f, 0.f, 0.f};
  SB();
#pragma unroll
  for (int s = 0; s < 16; ++s) {
    if (s + XW < 16) xf[s + XW] = LDX(s + XW);
    if (IS_B && s == HS0) hfl = __builtin_amdgcn_raw_buffer_load_b64(x.rs, fh * FSTR, WS_FLAGS, AUX_SC1);
    if (IS_B && s == HS1) {
#pragma unroll
      for (int i = 0; i < 2; ++i) hx[i] = ld128(x, hxv + i * 1024, hbase);
    }
    if (s == 4) sp = ld_stats(x, sv, sbase);
    if (IS_B && s == 10) hsp = ld_stats(x, w < 2 ? hsv : 0x7fffff00, WS_XS + L * XS_BUF);
    if (s + 1 < 16) {
#pragma unroll
      for (int ct = C0; ct < 6; ++ct) wf[(s + 1) & 1][ct] = WFRAG(s + 1, ct);
    }
#pragma unroll
    for (int ct = C0; ct < 6; ++ct) acc[ct] = mfma32(wf[s & 1][ct], xf[s], acc[ct]);
    if (IS_B) {
      if (s == HS2 || s == HS2 + 1) {
#pragma unroll
        for (int i = 0; i < 4; ++i) hacc[i] = mfma32(hf[i], hx[s - HS2], s == HS2 ? f32x4{0.f, 0.f, 0.f, 0.f} : hacc[i]);
      }
      if (s == HS2 - 1 || s == HS2) {
#pragma unroll
        for (int i = 0; i < 4; ++i) hf[i] = *(LAS u32x4*)(lds_at(wa + w * 8192) + (s - HS2 + 1) * 4096 + i * 1024);
      }
      if (s == HS2 + 2) {
#pragma unroll
        for (int i = 0; i < 4; ++i) *(LAS f32x4*)(lds_at(HP + w * 4096 + lane * 16) + i * 1024) = hacc[i];
      }
      if (s == HS2 + 3) *(volatile LAS u32x2*)lds_at(LDS_HF + w * 8) = u32x2{x.tag0, x.tag1 + L};
    }
    if (s == 15) {
#pragma unroll
      for (int ct = C0; ct < 6; ++ct) pre.cs[ct] = *(LAS f32x4*)(lds_at(LDS_CS + g * 16) + ct * 64);
    }
    if (s == 10) { OPAQUE_V(sp); finish_stats(sp, mean, rstd); }
    {
#pragma unroll
      for (int i = C0; i < 6; ++i) {
        __builtin_amdgcn_sched_group_barrier(0x008, 1, 0);
        __builtin_amdgcn_sched_group_barrier(0x100, 1, 0);
        if (s == 10) __builtin_amdgcn_sched_group_barrier(0x002, 5, 0);
        if (IS_B && s == HS2 + 2) __builtin_amdgcn_sched_group_barrier(0x200, 1, 0);
      }
      if (IS_B && s >= HS2 - 1 && s <= HS2 + 1) {
#pragma unroll
        for (int i = 0; i < 4; ++i) {
          __builtin_amdgcn_sched_group_barrier(0x008, 1, 0);
          __builtin_amdgcn_sched_group_barrier(0x100, 1, 0);
        }
      }
    }
    SB();
    if (IS_B && s == HS1 - 1) {
      if (__builtin_expect(!__all(hfl.x == x.tag0 && hfl.y == x.tag1), 0)) poll<0>(x, fh);
      asm volatile("" ::: "memory");
    }
  }
#undef LDX
  if (L == 1) {
    if (w < 4) {
#pragma unroll
      for (int n = 0; n < 8; ++n) poB[n] = ld128(x, lane * 16 + (n & 3) * 1024, WS_WOT + (16 + gj) * 65536 + w * 8192 + (n >> 2) * 4096);
    }
  }
}

template <int L, class F>
DI void mix_phase(const Ctx& x, const u32x4 (&wp)[4], u32x4 (&po)[8], const u32x4 (&xf)[16], f32x4 (&acc)[6], float mean, float rstd, u32x4 hsp,
                  const Pre& pre, F&& late_stores) {
  constexpr bool IS_B = L & 1;
  constexpr int KVW = IS_B ? LDS_Y : LDS_X, SCR = IS_B ? LDS_X : LDS_Y;
  constexpr int LDS_HF = SCR + 4096, LDS_S = SCR + 4096 + 64;
  int lane = x.lane, w = x.wave, gi = x.gi, gj = x.gj;
  OPAQUE_V(lane); OPAQUE_S(w); OPAQUE_S(gi); OPAQUE_S(gj);
  const int c = lane & 15, g = lane >> 4;
  const int LDS_CS = L < 2 ? SCR + 61440 : SCR + w * 512;
  const int tau = 8 * gi + w;
  const bool has_halo = IS_B && gi > 0;
  constexpr int KIM = LDS_S + 2048, VIMB = KIM + 8192, HP = VIMB + 10240;
  const int wa = KVW + lane * 16, wq = LDS_Z + lane * 16;
  constexpr int C0 = IS_B ? 0 : 2;
  const int q = c >> 2, p = c & 3;
  const u32x4 ones = {0x3f803f80u, 0x3f803f80u, 0x3f803f80u, 0x3f803f80u};
  const f32x4 (&cs)[6] = pre.cs;
#define STAGE_WO() do { _Pragma("unroll") for (int n = 0; n < 8; ++n) *(LAS u32x4*)(lds_at(KVW + w * 8192 + lane * 16) + n * 1024) = po[n]; } while (0)
  if (!IS_B) {
    constexpr int PHI = LDS_S, VIM = PHI + 16384, KVOP = VIM + 8192, ZOP = KVOP + 2048;
    {
      u32x2 kn[2];
#pragma unroll
      for (int h = 0; h < 2; ++h) kn[h] = ln_pack(acc[2 + h], cs[2 + h], mean, rstd);
      unsigned ph[8];
      feature_map(u32x4{kn[0].x, kn[0].y, kn[1].x, kn[1].y}, wp, ph, [&]() {
#pragma unroll
        for (int h = 0; h < 2; ++h) *(LAS u32x2*)(lds_at(pre.a1) + h * 4096) = ln_pack(acc[4 + h], cs[4 + h], mean, rstd);
      });
      *(LAS u32x4*)lds_at(pre.a0) = u32x4{ph[0], ph[1], ph[2], ph[3]};
      *(LAS u32x4*)(lds_at(pre.a0) + 16) = u32x4{ph[4], ph[5], ph[6], ph[7]};
    }
    constexpr int e = L >> 1;
    int kvo = WS_KV + e * KV_BUF + ((gj * 16 + gi) * 4 + w) * 1024 + lane * 16;
    int kzo = c == 0 ? WS_KZ + e * KZ_BUF + (gj * 4 + w) * 1024 + gi * 64 + g * 16 : 0x7ffffff0;
    int pa = pre.a2 + w * 4096, pb = pre.a2 + 16384 + (w >> 1) * 4096;
    u32x4 one4 = ones;
    OPAQUE_V(kvo); OPAQUE_V(kzo); OPAQUE_V(pa); OPAQUE_V(pb); OPAQUE_V(one4);
    if (w < 4) __builtin_amdgcn_s_setprio(2);
    BAR();
    if (w < 4) {
      f32x4 kv0 = {0.f, 0.f, 0.f, 0.f}, z0 = kv0;
      u32x4 a[4], b[4];
#pragma unroll
      for (int u = 0; u < 4; ++u) { a[u] = tr_frag(lds_at(pa) + u * 1024, 512); b[u] = tr_frag(lds_at(pb) + u * 1024, 512); }
      SB();
#pragma unroll
      for (int u = 0; u < 4; ++u) { kv0 = mfma32(a[u], b[u], kv0); z0 = mfma32(a[u], one4, z0); }
      st128(x, __builtin_bit_cast(u32x4, kv0), kvo, 0);
      st128(x, __builtin_bit_cast(u32x4, z0), kzo, 0);
      __builtin_amdgcn_s_setprio(0);
      if (L > 0) STAGE_WO();
      signal(x, lane, (e * 64 + gj * 4 + w) * 16 + gi);
      if (L == 0) late_stores();
    } else {
      __builtin_amdgcn_s_setprio(1);
    }
    if (L == 0) {
#pragma unroll
      for (int n = 0; n < 8; ++n) po[n] = ld128(x, lane * 16 + (n & 3) * 1024, WS_WOT + gj * 65536 + w * 8192 + (n >> 2) * 4096);
      SB();
    }
    u32x4 pq[2];
    {
      constexpr int QR = 6;
      u32x4 f[QR];
      f32x4 csq[2];
#pragma unroll
      for (int i = 0; i < QR; ++i) f[i] = WFRAG(i >> 1, i & 1);
#pragma unroll
      for (int i = 0; i < 32; ++i) {
        acc[i & 1] = mfma32(f[i % QR], xf[i >> 1], acc[i & 1]);
        if (i + QR < 32) f[i % QR] = WFRAG((i + QR) >> 1, (i + QR) & 1);
        else if (i + QR < 34) csq[i + QR - 32] = *(LAS f32x4*)(lds_at(LDS_CS + g * 16) + (i + QR - 32) * 64);
        SB();
      }
      u32x2 qn[2];
#pragma unroll
      for (int h = 0; h < 2; ++h) qn[h] = ln_pack(acc[h], csq[h], mean, rstd);
      unsigned ph[8];
      feature_map(u32x4{qn[0].x, qn[0].y, qn[1].x, qn[1].y}, wp, ph, []() {});
#pragma unroll
      for (int r = 0; r < 2; ++r) { swap16(ph[r], ph[2 + r]); swap16(ph[4 + r], ph[6 + r]); }
#pragma unroll
      for (int r = 0; r < 2; ++r) { swap32(ph[r], ph[4 + r]); swap32(ph[2 + r], ph[6 + r]); }
      pq[0] = u32x4{ph[0], ph[1], ph[2], ph[3]};
      pq[1] = u32x4{ph[4], ph[5], ph[6], ph[7]};
      OPAQUE_V(pq[0]); OPAQUE_V(pq[1]);
    }
    if (w >= 4) {
      const int wi = w - 4;
      int lv = lane * 16;
      int sbase = WS_KV + e * KV_BUF + (gj * 64 + wi) * 1024;
      int wr = KVOP + (wi >> 1) * 1024 + lane * 16 + (wi & 1) * 8;
      OPAQUE_S(sbase); OPAQUE_V(lv); OPAQUE_V(wr);
      poll<PRE_KV>(x, (e * 64 + gj * 4 + wi) * 16 + c);
      const u32x4 zu = ld128(x, lv, WS_KZ + e * KZ_BUF + (gj * 4 + wi) * 1024);
      u32x4 a[16];
#pragma unroll
      for (int i = 0; i < 16; ++i) { a[i] = ld128(x, lv, sbase + i * 4096); SB(); }
      STAGE_WO();
      const f32x4 zs = sum16q(zu);
      if (lane < 4) *(LAS u32x2*)lds_at(ZOP + (wi >> 1) * 64 + lane * 16 + (wi & 1) * 8) = u32x2{pk2(zs.x, zs.y), pk2(zs.z, zs.w)};
      f32x4 t[4];
#pragma unroll
      for (int i = 0; i < 4; ++i)
        t[i] = (__builtin_bit_cast(f32x4, a[4 * i]) + __builtin_bit_cast(f32x4, a[4 * i + 1])) + (__builtin_bit_cast(f32x4, a[4 * i + 2]) + __builtin_bit_cast(f32x4, a[4 * i + 3]));
      const f32x4 s0 = (t[0] + t[1]) + (t[2] + t[3]);
      *(LAS u32x2*)lds_at(wr) = u32x2{pk2(s0.x, s0.y), pk2(s0.z, s0.w)};
      __builtin_amdgcn_s_setprio(0);
    } else if (L == 0) {
      if (w < 3) signal(x, lane, (EDGE_SETUP * 128 + 64 + gj * 4 + w) * 16 + gi);
      STAGE_WO();
    }
    int za = ZOP + g * 16, ka = KVOP + lane * 16;
    OPAQUE_V(za); OPAQUE_V(ka);
    BAR();
    {
      const f32x4 z = {0.f, 0.f, 0.f, 0.f};
      u32x4 kvop[2], zop[2];
#pragma unroll
      for (int h = 0; h < 2; ++h) zop[h] = *(LAS u32x4*)(lds_at(za) + h * 64);
#pragma unroll
      for (int h = 0; h < 2; ++h) kvop[h] = *(LAS u32x4*)(lds_at(ka) + h * 1024);
      SB();
      f32x4 num[2], den[2];
#pragma unroll
      for (int h = 0; h < 2; ++h) den[h] = mfma32(zop[h], pq[h], z);
#pragma unroll
      for (int h = 0; h < 2; ++h) num[h] = mfma32(kvop[h], pq[h], z);
      SB();
      unsigned o[4];
#pragma unroll
      for (int h = 0; h < 2; ++h) {
        const float r = __builtin_amdgcn_rcpf(den[h].x + 1e-6f);
        o[2 * h] = pk2(num[h].x * r, num[h].y * r); o[2 * h + 1] = pk2(num[h].z * r, num[h].w * r);
      }
      st128(x, u32x4{o[0], o[1], o[2], o[3]}, pre.ov, 0);
    }
  } else {
    constexpr int VIM = VIMB;
    u32x2 qn[2], kn[2], kp[2];
#pragma unroll
    for (int h = 0; h < 2; ++h) {
      kn[h] = ln_pack(acc[2 + h], cs[2 + h], mean, rstd);
      *(LAS u32x2*)(lds_at(pre.a0) + h * 512) = kn[h];
      *(LAS u32x2*)(lds_at(pre.a1) + h * 512) = ln_pack(acc[4 + h], cs[4 + h], mean, rstd);
    }
    if (w < 2) {
      for (unsigned spins = 0; spins < SPIN_CAP; ++spins) {
        const u32x2 v = *(volatile LAS u32x2*)lds_at(LDS_HF + (lane & 7) * 8);
        if (__all(v.x == x.tag0 && v.y == x.tag1 + L)) break;
      }
      asm volatile("" ::: "memory");
      float hm, hr;
      finish_stats(hsp, hm, hr);
      const int src = HP + (c < 8 ? 2048 : 0) + (g * 16 + w * 8 + (c & 7)) * 16;
#pragma unroll
      for (int h = 0; h < 2; ++h) {
        f32x4 a[8];
#pragma unroll
        for (int i = 0; i < 8; ++i) a[i] = *(LAS f32x4*)(lds_at(src) + i * 4096 + h * 1024);
        const f32x4 sum = ((a[0] + a[1]) + (a[2] + a[3])) + ((a[4] + a[5]) + (a[6] + a[7]));
        const f32x4 csw = c < 8 ? cs[4 + h] : cs[2 + h];
        u32x2 v = ln_pack(sum, csw, hm, hr);
        if (!has_halo) v = u32x2{0u, 0u};
        kp[h] = c < 8 ? u32x2{0u, 0u} : v;
        *(LAS u32x2*)lds_at(VIM + (w * 2 + h) * 512 + (c ^ 8) * 32 + g * 8) = c < 8 ? v : u32x2{0u, 0u};
      }
    }
#pragma unroll
    for (int h = 0; h < 2; ++h) qn[h] = ln_pack(acc[h], cs[h], mean, rstd);
    f32x4 so[2];
#pragma unroll
    for (int h = 0; h < 2; ++h) so[h] = mfma16(kn[h], qn[h], pre.bo);
    if (w < 2) {
#pragma unroll
      for (int h = 0; h < 2; ++h) *(LAS u32x2*)(lds_at(pre.a0 - 2048) + h * 512) = kp[h];
    }
    SB();
    BAR();
    {
      const f32x4 z = {0.f, 0.f, 0.f, 0.f};
      const float SC = 0.25f * 1.4426950408889634f;
      f32x4 sp_[2];
      u32x2 vo[2], vp[2], kq[2];
      float m[2];
#pragma unroll
      for (int h = 0; h < 2; ++h) kq[h] = *(LAS u32x2*)(lds_at(pre.a0 - 2048) + h * 512);
#pragma unroll
      for (int h = 0; h < 2; ++h) {
        vp[h] = tr_read(lds_at(pre.a2) + h * 512);
        vo[h] = tr_read(lds_at(pre.a2) + 2048 + h * 512);
      }
      SB();
#pragma unroll
      for (int h = 0; h < 2; ++h) sp_[h] = mfma16(kq[h], qn[h], pre.bp);
#pragma unroll
      for (int h = 0; h < 2; ++h)
        m[h] = max3(max3(so[h].x, so[h].y, so[h].z), max3(so[h].w, sp_[h].x, sp_[h].y), max3(sp_[h].z, sp_[h].w, so[h].x));
      swap32f(m[0], m[1]);
      float t = max2(m[0], m[1]), u = t;
      swap16f(t, u);
      float a = max2(t, u), b = a;
      swap32f(a, b);
      m[0] = a; m[1] = b;
      u32x4 P[2];
#pragma unroll
      for (int h = 0; h < 2; ++h) {
        const f32x4 d0 = (so[h] - m[h]) * SC, d1 = (sp_[h] - m[h]) * SC;
        float e[8];
#pragma unroll
        for (int r = 0; r < 4; ++r) { e[r] = __builtin_amdgcn_exp2f(d0[r]); e[4 + r] = __builtin_amdgcn_exp2f(d1[r]); }
        P[h] = u32x4{pk2(e[0], e[1]), pk2(e[2], e[3]), pk2(e[4], e[5]), pk2(e[6], e[7])};
      }
      SB();
      f32x4 num[2], den[2];
#pragma unroll
      for (int h = 0; h < 2; ++h) den[h] = mfma32(ones, P[h], z);
#pragma unroll
      for (int h = 0; h < 2; ++h) num[h] = mfma32(u32x4{vo[h].x, vo[h].y, vp[h].x, vp[h].y}, P[h], z);
      SB();
      unsigned o[4];
#pragma unroll
      for (int h = 0; h < 2; ++h) {
        const float r = __builtin_amdgcn_rcpf(den[h].x);
        o[2 * h] = pk2(num[h].x * r, num[h].y * r); o[2 * h + 1] = pk2(num[h].z * r, num[h].w * r);
      }
      st128(x, u32x4{o[0], o[1], o[2], o[3]}, pre.ov, 0);
    }
    SB();
    STAGE_WO();
  }
  signal(x, lane, ((EDGE_O + L) * 128 + tau) * 16 + gj);
  BAR();
#undef WFRAG
#undef STAGE_WO
}

template <int L>
DI void oproj_phase(const Ctx& x, f32x4 (&res)[4], u32x4 (&poB)[8], u16* out) {
  constexpr bool IS_B = L & 1;
  constexpr int KVW = IS_B ? LDS_Y : LDS_X, SCR = IS_B ? LDS_X : LDS_Y;
  int lane = x.lane, w = x.wave, gi = x.gi, gj = x.gj;
  OPAQUE_V(lane); OPAQUE_S(w); OPAQUE_S(gi); OPAQUE_S(gj);
  const int c = lane & 15, g = lane >> 4;
  const int tau = 8 * gi + 4 * (gj >> 3) + w;
  f32x4 cb[2] = {f32x4{0.f, 0.f, 0.f, 0.f}, f32x4{0.f, 0.f, 0.f, 0.f}};
  if (w < 4) {
    const int wb = KVW + lane * 16;
    constexpr int PD = 3, OW = OWIN;
    u32x4 wf[PD][4];
#pragma unroll
    for (int s = 0; s < PD; ++s)
#pragma unroll
      for (int ct = 0; ct < 4; ++ct) wf[s][ct] = *(LAS u32x4*)(lds_at(wb) + s * 4096 + ct * 1024);
    int xo = WS_XF + (L + 1) * FRAG_BUF + (tau * 16 + (gj & 7) * 2) * 1024 + lane * 16;
    int so = g == (c >> 2) ? WS_XS + (L + 1) * XS_BUF + tau * 1024 + (gj & 7) * 128 + c * 8 : 0x7ffffff0;
    u32x4 one4 = {0x3f803f80u, 0x3f803f80u, 0x3f803f80u, 0x3f803f80u};
    int obase = WS_OF + L * FRAG_BUF + tau * 16384, lv = lane * 16;
    if (L < 3) { OPAQUE_V(xo); OPAQUE_V(so); OPAQUE_V(one4); }
    OPAQUE_S(obase); OPAQUE_V(lv);
    poll<(IS_B ? PRE_OB : PRE_OA)>(x, ((EDGE_O + L) * 128 + tau) * 16 + c);
    u32x4 of[16];
#pragma unroll
    for (int s = 0; s < OW; ++s) { of[s] = ld128(x, lv + (s & 3) * 1024, obase + (s >> 2) * 4096); SB(); }
#pragma unroll
    for (int s = 0; s < 16; ++s) {
      if (s + OW < 16) of[s + OW] = ld128(x, lv + ((s + OW) & 3) * 1024, obase + ((s + OW) >> 2) * 4096);
#pragma unroll
      for (int ct = 0; ct < 4; ++ct) {
        res[ct] = mfma32(wf[s % PD][ct], of[s], res[ct]);
        if (s + PD < 16) wf[s % PD][ct] = *(LAS u32x4*)(lds_at(wb) + (s + PD) * 4096 + ct * 1024);
      }
      SB();
    }
    const int nb = gj & 7;
    u32x4 f[2];
#pragma unroll
    for (int a = 0; a < 2; ++a)
      f[a] = u32x4{pk2(res[2 * a].x, res[2 * a].y), pk2(res[2 * a].z, res[2 * a].w), pk2(res[2 * a + 1].x, res[2 * a + 1].y), pk2(res[2 * a + 1].z, res[2 * a + 1].w)};
    if (L < 3) {
      const f32x4 z = {0.f, 0.f, 0.f, 0.f};
      const f32x4 sq = mfma32(f[1], f[1], mfma32(f[0], f[0], z)), sm = mfma32(one4, f[1], mfma32(one4, f[0], z));
#pragma unroll
      for (int a = 0; a < 2; ++a) st128(x, f[a], xo + a * 1024, 0);
      st64(x, u32x2{__float_as_uint(sm.x), __float_as_uint(sel4(sq, c & 3))}, so, 0);
      signal(x, lane, ((EDGE_X + L + 1) * 128 + tau) * 16 + nb);
    } else {
      const int tok = 32 * (tau >> 1) + 2 * c + (tau & 1);
#pragma unroll
      for (int a = 0; a < 2; ++a) {
        *(GAS u32x4*)(out + tok * 512 + nb * 64 + a * 32 + 8 * g) = f[a];
      }
    }
  } else if (L < 3) {
    if (!IS_B && L == 0) {
      poll<0>(x, (EDGE_SETUP * 128 + 64 + gj * 4) * 16 + (lane < 48 ? lane : 0));
#pragma unroll
      for (int n = 0; n < 2; ++n) cb[n] = sum16q(ld128(x, lane * 16, WS_CSP + (16 + gj) * 6144 + ((w < 7 ? w - 4 : 0) * 2 + n) * 1024));
    }
    const int src = WS_WT + ((IS_B ? 0 : 16) + gj) * 98304;
    const int wi = w - 4;
    u32x4 f[24];
#define FSRC(n) ((n) < 16 ? src + wi * 16384 + ((n) >> 2) * 4096 : src + 65536 + wi * 8192 + (((n) - 16) >> 2) * 4096)
#define FDST(n) ((n) < 16 ? lds_at(SCR + wi * 16384 + lane * 16) + (n) * 1024 : lds_at(LDS_Z + wi * 8192 + lane * 16) + ((n) - 16) * 1024)
#pragma unroll
    for (int n = 0; n < 24; ++n) {
      f[n] = ld128(x, lane * 16 + (n & 3) * 1024, FSRC(n));
      __builtin_amdgcn_s_sleep(DMA_SLEEP);
      if (n >= 6) *(LAS u32x4*)FDST(n - 6) = f[n - 6];
      SB();
    }
#pragma unroll
    for (int n = 18; n < 24; ++n) *(LAS u32x4*)FDST(n) = f[n];
#undef FSRC
#undef FDST
  }
  if (!IS_B && L == 0) {
    if (w >= 4) {
#pragma unroll
      for (int n = 0; n < 8; ++n) poB[n] = ld128(x, lane * 16 + (n & 3) * 1024, WS_WOT + (16 + gj) * 65536 + w * 8192 + (n >> 2) * 4096);
    }
  }
  if (L < 3) BAR();
  if (L == 0 && w >= 4 && w < 7) {
    if (lane < 4) {
#pragma unroll
      for (int n = 0; n < 2; ++n) *(LAS f32x4*)lds_at(KVW + 61440 + ((w - 4) * 2 + n) * 64 + lane * 16) = cb[n];
    }
    *(volatile LAS u32x2*)lds_at(KVW + 61952 + (w - 4) * 8) = u32x2{x.tag0, x.tag1};
  }
}

__global__ void __launch_bounds__(512, 2)
fused_fwd(const u16* __restrict__ x0, const u16* __restrict__ wA, const u16* __restrict__ wphi, const u16* __restrict__ woA,
          const u16* __restrict__ wB, const u16* __restrict__ woB, u16* __restrict__ out, unsigned char* __restrict__ wsp) {
  extern __shared__ __attribute__((aligned(16))) unsigned char smem[];
  asm volatile("" : "+s"(x0), "+s"(wA), "+s"(wphi), "+s"(woA), "+s"(wB), "+s"(woB), "+s"(out), "+s"(wsp));
  Ctx x;
  x.lane = threadIdx.x & 63;
  x.wave = __builtin_amdgcn_readfirstlane(threadIdx.x >> 6);
  {
    constexpr int NJ = MAP_NJ, NI = 32 / NJ, XJ = 16 / NJ;
    const int xc = blockIdx.x & 7, r = blockIdx.x >> 3;
    x.gi = (xc / XJ) * NI + r / NJ;
    x.gj = (xc % XJ) * NJ + r % NJ;
  }
  x.rs = __builtin_amdgcn_make_buffer_rsrc((void*)wsp, 0, WS_END, 0x00020000);
  const int lane = x.lane, w = x.wave, c = lane & 15, g = lane >> 4, q = c >> 2, p = c & 3, gi = x.gi, gj = x.gj;
  const u32x4 ones = {0x3f803f80u, 0x3f803f80u, 0x3f803f80u, 0x3f803f80u};
  const int tau0 = 8 * gi + w;
  const int xstg = (w < 4 ? LDS_Y : LDS_X) + (w & 3) * 16384;
  f32x4 res[4];
  auto unit_dma = [&](int u, int slot) {
    const bool isw = u < 6;
    const char* src = isw ? (const char*)(u < 3 ? wA : wB) + (size_t)gi * 98304 + (u % 3) * 1024 + gj * 64
                          : (const char*)(u < 8 ? woA : woB) + (size_t)gi * 32768 + (gj & 7) * 128 + (u & 1) * 64;
    const int ld = isw ? 3072 : 1024;
    const unsigned vo = (lane >> 2) * ld + (lane & 3) * 16;
    dma16(src, vo, slot);
    dma16(src + 16 * ld, vo, slot + 1024);
  };
  auto unit_read = [&](int u, int slot, u32x4 (&f)[2]) {
    const bool isw = u < 6;
    const int ad = slot + (isw ? (8 * g + q) * 64 + p * 8 : (4 * g + q) * 64 + p * 16);
    const int sec = isw ? 256 : 1024, astr = isw ? 32 : 8;
#pragma unroll
    for (int a = 0; a < 2; ++a) {
      const u32x2 lo = tr_read(lds_at(ad + a * astr)), hi = tr_read(lds_at(ad + a * astr + sec));
      f[a] = u32x4{lo.x, lo.y, hi.x, hi.y};
    }
  };
  auto unit_store = [&](int u, const u32x4 (&f)[2]) {
    const bool isw = u < 6;
    const int s3 = u % 3;
    const int dst = isw ? WS_WT + ((u / 3) * 16 + gj) * 98304 + (s3 == 0 ? 65536 + gi * 2048 : gi * 4096 + (s3 - 1) * 2048)
                        : WS_WOT + (((u - 6) >> 1) * 16 + gj) * 65536 + gi * 4096 + (u & 1) * 2048;
#pragma unroll
    for (int a = 0; a < 2; ++a) st128(x, f[a], lane * 16 + a * 1024, dst);
    if (isw) {
      const f32x4 s0 = mfma32(f[0], ones, f32x4{0.f, 0.f, 0.f, 0.f}), s1 = mfma32(f[1], ones, f32x4{0.f, 0.f, 0.f, 0.f});
      if (c < 2) st128(x, __builtin_bit_cast(u32x4, c ? s1 : s0), g * 16 + c * 1024, WS_CSP + (((u / 3) * 16 + gj) * 6 + s3 * 2) * 1024 + gi * 64);
    }
  };
  auto load_tag = [&]() {
    unsigned long long t;
    asm volatile("s_load_dwordx2 %0, %1, 0x0\n\ts_waitcnt lgkmcnt(0)" : "=s"(t) : "s"(wsp + WS_EPOCH) : "memory");
    unsigned long long z = t + 0x9E3779B97F4A7C15ull;
    z = (z ^ (z >> 30)) * 0xBF58476D1CE4E5B9ull;
    z = (z ^ (z >> 27)) * 0x94D049BB133111EBull;
    z ^= z >> 31;
    if (z == t) z = ~z;
    x.tag0 = (unsigned)z; x.tag1 = (unsigned)(z >> 32);
  };
  const int zs = LDS_Z + (w & 3) * 8192;
  if (w < 4) {
    const int u0 = w < 3 ? w : 6;
    unit_dma(u0, zs);
    if (w == 3) unit_dma(7, zs + 2048);
    load_tag();
    DRAIN();
    u32x4 f[2];
    unit_read(u0, zs, f);
    unit_store(u0, f);
    if (w == 3) { unit_read(7, zs + 2048, f); unit_store(7, f); }
    signal(x, lane, (EDGE_SETUP * 128 + gj * 4 + w) * 16 + gi);
  } else {
    if (w == 7) {
      unsigned long long pc; unsigned len;
      asm volatile("s_getpc_b64 %0\n.Lfused_fwd_here:\n\ts_mov_b32 %1, .Lfused_fwd_end-.Lfused_fwd_here" : "=s"(pc), "=s"(len));
      const unsigned off = ((blockIdx.x >> 3) + 32 * (lane & 15)) * 128;
      dma16((const void*)(pc & ~15ull), off + 16 <= len && (int)len > 0 ? off : 0u, zs + 4096);
    }
  }
  {
    const char* xrow = (const char*)x0 + (32 * (tau0 >> 1) + (tau0 & 1)) * 1024;
#pragma unroll
    for (int n = 0; n < 16; ++n) dma16(xrow + n * 2048, (lane ^ (n << 2)) << 4, xstg + n * 1024);
  }
  if (w < 4) {
    if (w < 3) unit_dma(3 + w, zs + 2048);
    if (w < 2) unit_dma(8 + w, zs + 4096);
    const int tau = 8 * gi + 4 * (gj >> 3) + w;
    const unsigned rvo = (32 * (tau >> 1) + 2 * (lane >> 3) + (tau & 1)) * 1024 + ((lane & 7) << 4);
    dma16((const char*)x0 + (gj & 7) * 128, rvo, zs + 6144);
    dma16((const char*)x0 + (gj & 7) * 128 + 16384, rvo, zs + 7168);
    dma16((const char*)wphi + gj * 1024, lane * 16, zs);
  }
  u32x4 wp[4];
  auto read_wp = [&]() {
    const unsigned sg = (q & 1) ? 0x80008000u : 0u;
#pragma unroll
    for (int t = 0; t < 4; ++t) {
      const u32x2 v = tr_read(lds_at(zs + (p >> 1) * 512 + (4 * g + q) * 32) + t * 8);
      wp[t] = (q >> 1) ? u32x4{0u, 0u, v.x ^ sg, v.y ^ sg} : u32x4{v.x ^ sg, v.y ^ sg, 0u, 0u};
    }
    OPAQUE_V(wp[0]); OPAQUE_V(wp[1]); OPAQUE_V(wp[2]); OPAQUE_V(wp[3]);
  };
  if (w >= 4) load_tag();
  DRAIN();
  u32x4 xf[16], fd0[2], fd1[2];
#pragma unroll
  for (int s = 0; s < 16; ++s) xf[s] = *(LAS u32x4*)lds_at(xstg + c * 1024 + ((s ^ c) << 6) + g * 16);
  unit_read(3, zs + 2048, fd0);
  unit_read(8, zs + 4096, fd1);
#pragma unroll
  for (int a = 0; a < 2; ++a) {
    const u32x4 v = *(LAS u32x4*)lds_at(zs + 6144 + c * 128 + ((a * 4 + g) << 4));
    res[2 * a] = f32x4{bflo(v.x), bfhi(v.x), bflo(v.y), bfhi(v.y)};
    res[2 * a + 1] = f32x4{bflo(v.z), bfhi(v.z), bflo(v.w), bfhi(v.w)};
  }
  asm volatile("s_waitcnt lgkmcnt(0)" : "+v"(xf[0]), "+v"(xf[1]), "+v"(xf[2]), "+v"(xf[3]), "+v"(xf[4]), "+v"(xf[5]), "+v"(xf[6]), "+v"(xf[7]),
               "+v"(xf[8]), "+v"(xf[9]), "+v"(xf[10]), "+v"(xf[11]), "+v"(xf[12]), "+v"(xf[13]), "+v"(xf[14]), "+v"(xf[15]),
               "+v"(fd0[0]), "+v"(fd0[1]), "+v"(fd1[0]), "+v"(fd1[1]), "+v"(res[0]), "+v"(res[1]), "+v"(res[2]), "+v"(res[3]) :: "memory");
  float mean, rstd;
  auto row_stats = [&]() {
    f32x4 accs = {0.f, 0.f, 0.f, 0.f}, accq = accs;
#pragma unroll
    for (int n = 0; n < 16; ++n) { accs = mfma32(ones, xf[n], accs); accq = mfma32(xf[n], xf[n], accq); }
    const float dq = sel4(accq, p);
    finish_stats(u32x4{__float_as_uint(accs.x * 0.25f), g == q ? __float_as_uint(dq) : 0u, 0u, 0u}, mean, rstd);
    OPAQUE_V(mean); OPAQUE_V(rstd);
  };
  row_stats();
  u32x4 fq[4];
  f32x4 cb[2];
  if (w >= 4) {
    poll<0>(x, (EDGE_SETUP * 128 + gj * 4) * 16 + lane);
    u32x4 f[16], cp[2];
#pragma unroll
    for (int n = 0; n < 16; ++n) { f[n] = ld128(x, lane * 16 + (n & 3) * 1024, WS_WT + gj * 98304 + (w - 4) * 16384 + (n >> 2) * 4096); SB(); }
#pragma unroll
    for (int n = 0; n < 2; ++n) cp[n] = ld128(x, lane * 16, WS_CSP + gj * 6144 + ((w < 7 ? w - 4 : 0) * 2 + n) * 1024);
    SB();
#pragma unroll
    for (int n = 0; n < 16; ++n) *(LAS u32x4*)(lds_at(xstg + lane * 16) + n * 1024) = f[n];
#pragma unroll
    for (int n = 0; n < 2; ++n) cb[n] = sum16q(cp[n]);
  } else {
    cb[0] = f32x4{0.f, 0.f, 0.f, 0.f}; cb[1] = cb[0];
    read_wp();
  }
  BAR();
  if (w >= 4) {
    read_wp();
    if (w < 7 && lane < 4) {
#pragma unroll
      for (int n = 0; n < 2; ++n) *(LAS f32x4*)lds_at(LDS_Y + 61440 + ((w - 4) * 2 + n) * 64 + lane * 16) = cb[n];
    }
    if (w < 7) *(volatile LAS u32x2*)lds_at(LDS_Y + 61952 + (w - 4) * 8) = u32x2{x.tag0, x.tag1};
  }
  Pre pre;
  make_pre<0>(pre, lane, w, gi, gj);
  u32x2 rdy;
  const int qsl = w >= 4 ? (w - 4) * 2 : w * 2 + 1;
  u32x4 poA[8], poB[8];
  f32x4 acc[6];
#pragma unroll
  for (int ct = 0; ct < 6; ++ct) acc[ct] = f32x4{0.f, 0.f, 0.f, 0.f};
  {
    u32x4 wf[2][4];
#pragma unroll
    for (int ct = 0; ct < 4; ++ct) wf[0][ct] = *(LAS u32x4*)(lds_at(LDS_X + lane * 16) + ct * 1024);
    SB();
#pragma unroll
    for (int s = 0; s < 16; ++s) {
      if (s + 1 < 16) {
#pragma unroll
        for (int ct = 0; ct < 4; ++ct) wf[(s + 1) & 1][ct] = *(LAS u32x4*)(lds_at(LDS_X + lane * 16) + (s + 1) * 4096 + ct * 1024);
      }
#pragma unroll
      for (int ct = 0; ct < 4; ++ct) acc[2 + ct] = mfma32(wf[s & 1][ct], xf[s], acc[2 + ct]);
      if (s >= 1 && s < 5) fq[s - 1] = ld128(x, lane * 16 + (s - 1) * 1024, WS_WT + gj * 98304 + 65536 + qsl * 4096);
      if (s == 12) rdy = *(volatile LAS u32x2*)lds_at(LDS_Y + 61952 + (lane % 3) * 8);
      if (s + 1 < 16) {
#pragma unroll
        for (int i = 0; i < 4; ++i) {
          __builtin_amdgcn_sched_group_barrier(0x008, 1, 0);
          __builtin_amdgcn_sched_group_barrier(0x100, 1, 0);
        }
      }
      SB();
    }
  }
#pragma unroll
  for (int n = 0; n < 4; ++n) *(LAS u32x4*)(lds_at(LDS_Z + qsl * 4096 + lane * 16) + n * 1024) = fq[n];
  if (__builtin_expect(!__all(rdy.x == x.tag0 && rdy.y == x.tag1), 0)) {
    for (unsigned spins = 0; spins < SPIN_CAP; ++spins) {
      const u32x2 v = *(volatile LAS u32x2*)lds_at(LDS_Y + 61952 + (lane % 3) * 8);
      if (__all(v.x == x.tag0 && v.y == x.tag1)) break;
    }
  }
  asm volatile("" ::: "memory");
  f32x2 csA = {*(volatile LAS float*)lds_at(LDS_Y + 61440 + lane * 4), *(volatile LAS float*)lds_at(LDS_Y + 61440 + 256 + (lane & 31) * 4)};
  f32x2 csB = {0.f, 0.f};
  u32x4 hsp = {0u, 0u, 0u, 0u};
#pragma unroll
  for (int ct = 2; ct < 6; ++ct) pre.cs[ct] = *(volatile LAS f32x4*)(lds_at(LDS_Y + 61440 + g * 16) + ct * 64);
  auto none = []() {};
  mix_phase<0>(x, wp, poA, xf, acc, mean, rstd, hsp, pre, [&]() {
    if (w < 3) unit_store(3 + w, fd0);
    if (w < 2) unit_store(8 + w, fd1);
  });
  oproj_phase<0>(x, res, poB, out);
  if (blockIdx.x == 0 && threadIdx.x == 0) st64(x, u32x2{x.tag0, x.tag1}, 0, WS_EPOCH);
  gemm_phase<1>(x, csB, poB, xf, acc, mean, rstd, hsp, pre);
  mix_phase<1>(x, wp, poB, xf, acc, mean, rstd, hsp, pre, none);
  oproj_phase<1>(x, res, poB, out);
  gemm_phase<2>(x, csA, poB, xf, acc, mean, rstd, hsp, pre);
  mix_phase<2>(x, wp, poA, xf, acc, mean, rstd, hsp, pre, none);
  oproj_phase<2>(x, res, poB, out);
  gemm_phase<3>(x, csB, poB, xf, acc, mean, rstd, hsp, pre);
  mix_phase<3>(x, wp, poB, xf, acc, mean, rstd, hsp, pre, none);
  oproj_phase<3>(x, res, poB, out);
  asm volatile(".Lfused_fwd_end:" ::: "memory");
}

extern "C" void kernel_launch(void* const* d_in, const int* in_sizes, int n_in, void* d_out, int out_size, void* d_ws, size_t ws_size,
                              hipStream_t stream) {
  (void)in_sizes; (void)n_in; (void)out_size; (void)ws_size;
  (void)hipFuncSetAttribute((const void*)fused_fwd, hipFuncAttributeMaxDynamicSharedMemorySize, LDS_BYTES);
  fused_fwd<<<256, 512, LDS_BYTES, stream>>>((const u16*)d_in[0], (const u16*)d_in[1], (const u16*)d_in[2], (const u16*)d_in[3],
                                             (const u16*)d_in[5], (const u16*)d_in[6], (u16*)d_out, (unsigned char*)d_ws);
}
```
